```python
import torch

def _softmax(x, axis=-1):
    return torch.softmax(x, dim=axis)
def _gelu(x):
    return torch.nn.functional.gelu(x, approximate="tanh")
def _silu(x):
    return torch.nn.functional.silu(x)
def _sigmoid(x):
    return torch.sigmoid(x)
def _layernorm(x, axis=-1, eps=1e-5):
    m = x.mean(dim=axis, keepdim=True)
    v = x.var(dim=axis, keepdim=True, unbiased=False)
    return (x - m) / torch.sqrt(v + eps)
def _scatter_add(tgt, idx, src, axis=0):
    return tgt.clone().index_add_(axis, idx, src)
def _segment_sum(src, seg, n):
    out = torch.zeros((n,) + src.shape[1:], dtype=src.dtype)
    return out.index_add_(0, seg, src)
def _pad(x, pw):
    flat = tuple(v for pair in reversed(pw) for v in pair)
    return torch.nn.functional.pad(x, flat)

kernel_name = 'interleave_layers__sliding_block_cached__mixer_channel_mlp__decode'

def setup_inputs():

    _w = (lambda a: (lambda t: t.to(torch.bfloat16) if t.is_floating_point() else t)(torch.as_tensor(a)))
    def _A_setup():
        B, Sq, D = 8, 1, 1024
        Hq, Hkv, Sk, Dh = 32, 4, 4096, 128
        assert Hq % Hkv == 0, (Hq, Hkv)
        return {
            "x": _w((lambda *s: torch.randn(*s, dtype=torch.bfloat16) * 0.05)(B, Sq, D)),
            "k_cache": _w((lambda *s: torch.randn(*s, dtype=torch.bfloat16) * 1.6)(B, Hkv, Sk, Dh)),
            "v_cache": _w((lambda *s: torch.randn(*s, dtype=torch.bfloat16) * 1.6)(B, Hkv, Sk, Dh)),
            "wq": _w((lambda *s: torch.randn(*s, dtype=torch.bfloat16) * 1.6)(D, Hq * Dh) * (D ** -0.5)),
            "wo": _w((lambda *s: torch.randn(*s, dtype=torch.bfloat16) * 1.6)(Hq * Dh, D) * ((Hq * Dh) ** -0.5)),
        }
    _A_in = _A_setup()

    def _B_setup():
        B, S, D, F = 8, 1, 1024, 3584
        return {
            "x": _w((lambda *s: torch.randn(*s, dtype=torch.bfloat16) * 0.05)(B, S, D)),
            "w1": _w((lambda *s: torch.randn(*s, dtype=torch.bfloat16) * 3.2)(D, F) * (D ** -0.5)),
            "b1": _w((lambda *s: torch.randn(*s, dtype=torch.bfloat16) * 0.8)(F)),
            "w2": _w((lambda *s: torch.randn(*s, dtype=torch.bfloat16) * 0.4)(F, D) * (F ** -0.5)),
            "b2": _w((lambda *s: torch.randn(*s, dtype=torch.bfloat16) * 0.4)(D)),
        }
    _B_in = _B_setup()

    return {
        "A_x": _A_in["x"],
        "A_k_cache": _A_in["k_cache"],
        "A_v_cache": _A_in["v_cache"],
        "A_wq": _A_in["wq"],
        "A_wo": _A_in["wo"],
        "B_x": _B_in["x"],
        "B_w1": _B_in["w1"],
        "B_b1": _B_in["b1"],
        "B_w2": _B_in["w2"],
        "B_b2": _B_in["b2"],
    }

def reference(A_x, A_k_cache, A_v_cache, A_wq, A_wo, B_x, B_w1, B_b1, B_w2, B_b2):
    def _A_ref(x, k_cache, v_cache, wq, wo):
        B, Sq, D = x.shape
        Hq, Dh = 32, 128
        Hkv, Sk = k_cache.shape[1], k_cache.shape[2]
        W = min(4096, max(256, Sk // 4))
        rep = Hq // Hkv
        q = (lambda x, *d: x.permute(*d))((x @ wq).reshape(B, Sq, Hq, Dh), 0, 2, 1, 3)
        k = (lambda x, n, axis: torch.repeat_interleave(x, n, dim=axis))(k_cache[:, :, Sk - W:, :], rep, axis=1)
        v = (lambda x, n, axis: torch.repeat_interleave(x, n, dim=axis))(v_cache[:, :, Sk - W:, :], rep, axis=1)
        scores = torch.einsum("bhqd,bhkd->bhqk", q, k) * (Dh ** -0.5)
        p = _softmax(scores, axis=-1)
        o = torch.einsum("bhqk,bhkd->bhqd", p, v)
        return (lambda x, *d: x.permute(*d))(o, 0, 2, 1, 3).reshape(B, Sq, Hq * Dh) @ wo

    def _B_ref(x, w1, b1, w2, b2):
        y = _layernorm(x, axis=-1)
        h = _gelu(torch.einsum("bsd,df->bsf", y, w1) + b1)
        return torch.einsum("bsf,fd->bsd", h, w2) + b2

    x = A_x
    x = x + _A_ref(_layernorm(x, axis=-1), A_k_cache, A_v_cache, A_wq, A_wo)
    x = x + _B_ref(_layernorm(x, axis=-1), B_w1, B_b1, B_w2, B_b2)
    x = x + _A_ref(_layernorm(x, axis=-1), A_k_cache, A_v_cache, A_wq, A_wo)
    x = x + _B_ref(_layernorm(x, axis=-1), B_w1, B_b1, B_w2, B_b2)
    return x
```

```cpp
#include <hip/hip_runtime.h>
#include <hip/hip_bf16.h>
typedef unsigned short u16;
__device__ __forceinline__ float b2f(u16 v) { return __uint_as_float((unsigned)v << 16); }
__device__ __forceinline__ u16 f2b(float f) { unsigned u = __float_as_uint(f); u += 0x7fffu + ((u >> 16) & 1u); return (u16)(u >> 16); }
__device__ __forceinline__ float rb(float f) { return f; }
constexpr int B = 8, D = 1024, HQ = 32, HKV = 4, SK = 4096, DH = 128, W = 1024, F = 3584;

__global__ void k_ln(const float* x, float* xn, int dbl) {
  __shared__ float red[256];
  const int b = blockIdx.x, t = threadIdx.x;
  float v[4];
  for (int i = 0; i < 4; ++i) v[i] = x[b * D + t * 4 + i];
  for (int pass = 0; pass <= dbl; ++pass) {
    float s = v[0] + v[1] + v[2] + v[3];
    red[t] = s; __syncthreads();
    for (int o = 128; o > 0; o >>= 1) { if (t < o) red[t] += red[t + o]; __syncthreads(); }
    float m = red[0] / D; __syncthreads();
    float q = 0; for (int i = 0; i < 4; ++i) q += (v[i] - m) * (v[i] - m);
    red[t] = q; __syncthreads();
    for (int o = 128; o > 0; o >>= 1) { if (t < o) red[t] += red[t + o]; __syncthreads(); }
    float var = red[0] / D; __syncthreads();
    float r = 1.0f / sqrtf(var + 1e-5f);
    for (int i = 0; i < 4; ++i) v[i] = rb((v[i] - m) * r);
  }
  for (int i = 0; i < 4; ++i) xn[b * D + t * 4 + i] = v[i];
}
__global__ void k_gemv(const float* in, const u16* w, const u16* bias, float* out, int K, int N, int mode, u16* final_out) {
  __shared__ float red[4][8][64];
  const int n = blockIdx.x * 64 + (threadIdx.x & 63), ks = threadIdx.x >> 6;
  float acc[8] = {0, 0, 0, 0, 0, 0, 0, 0};
  for (int k = ks; k < K; k += 4) { float wv = b2f(w[(size_t)k * N + n]);
    for (int b = 0; b < 8; ++b) acc[b] += in[b * K + k] * wv; }
  for (int b = 0; b < 8; ++b) red[ks][b][threadIdx.x & 63] = acc[b];
  __syncthreads();
  if (ks == 0) for (int b = 0; b < 8; ++b) {
    float a = red[0][b][threadIdx.x] + red[1][b][threadIdx.x] + red[2][b][threadIdx.x] + red[3][b][threadIdx.x];
    if (mode == 0) out[b * N + n] = rb(a);
    else if (mode == 1) { float z = rb(rb(a) + b2f(bias[n])); float g = 0.5f * z * (1.0f + tanhf(0.7978845608f * (z + 0.044715f * z * z * z))); out[b * N + n] = rb(g); }
    else { float z = rb(a); if (bias) z = rb(z + b2f(bias[n])); float r = rb(out[b * N + n] + z); out[b * N + n] = r; if (final_out) final_out[b * N + n] = f2b(r); }
  }
}
__global__ void k_attn(const float* q, const u16* kc, const u16* vc, float* o) {
  __shared__ float s[W]; __shared__ float red[256]; __shared__ float qs[DH];
  const int b = blockIdx.x / HQ, h = blockIdx.x % HQ, g = h / (HQ / HKV), t = threadIdx.x;
  const u16* K = kc + ((size_t)(b * HKV + g) * SK + (SK - W)) * DH; const u16* V = vc + ((size_t)(b * HKV + g) * SK + (SK - W)) * DH;
  if (t < DH) qs[t] = q[b * HQ * DH + h * DH + t];
  __syncthreads();
  float mx = -INFINITY;
  for (int j = t; j < W; j += 256) { float a = 0; for (int d = 0; d < DH; ++d) a += qs[d] * b2f(K[(size_t)j * DH + d]); a = rb(rb(a) * 0.08838834764831845f); s[j] = a; mx = fmaxf(mx, a); }
  red[t] = mx; __syncthreads();
  for (int of = 128; of > 0; of >>= 1) { if (t < of) red[t] = fmaxf(red[t], red[t + of]); __syncthreads(); }
  mx = red[0]; __syncthreads();
  float sum = 0; for (int j = t; j < W; j += 256) { float e = expf(s[j] - mx); s[j] = e; sum += e; }
  red[t] = sum; __syncthreads();
  for (int of = 128; of > 0; of >>= 1) { if (t < of) red[t] += red[t + of]; __syncthreads(); }
  sum = red[0]; __syncthreads();
  if (t < DH) { float a = 0; for (int j = 0; j < W; ++j) a += rb(s[j] / sum) * b2f(V[(size_t)j * DH + t]); o[b * HQ * DH + h * DH + t] = rb(a); }
}
__global__ void k_init(const u16* x, float* xf) { int i = blockIdx.x * 256 + threadIdx.x; xf[i] = b2f(x[i]); }

extern "C" void kernel_launch(void* const* d_in, const int* in_sizes, int n_in, void* d_out, int out_size, void* d_ws, size_t ws_size, hipStream_t stream) {
  const u16* x = (const u16*)d_in[0]; const u16* kc = (const u16*)d_in[1]; const u16* vc = (const u16*)d_in[2];
  const u16* wq = (const u16*)d_in[3]; const u16* wo = (const u16*)d_in[4];
  const u16* w1 = (const u16*)d_in[6]; const u16* b1 = (const u16*)d_in[7]; const u16* w2 = (const u16*)d_in[8]; const u16* b2 = (const u16*)d_in[9];
  float* xf = (float*)d_ws; float* xn = xf + 8192; float* q = xn + 8192; float* o = q + 32768; float* h = o + 32768;
  k_init<<<32, 256, 0, stream>>>(x, xf);
  for (int l = 0; l < 2; ++l) {
    k_ln<<<8, 256, 0, stream>>>(xf, xn, 0);
    k_gemv<<<64, 256, 0, stream>>>(xn, wq, nullptr, q, D, HQ * DH, 0, nullptr);
    k_attn<<<256, 256, 0, stream>>>(q, kc, vc, o);
    k_gemv<<<16, 256, 0, stream>>>(o, wo, nullptr, xf, HQ * DH, D, 2, nullptr);
    k_ln<<<8, 256, 0, stream>>>(xf, xn, 1);
    k_gemv<<<56, 256, 0, stream>>>(xn, w1, b1, h, D, F, 1, nullptr);
    k_gemv<<<16, 256, 0, stream>>>(h, w2, b2, xf, F, D, 2, l == 1 ? (u16*)d_out : nullptr);
  }
}
```

```cpp
#include <hip/hip_runtime.h>
#include <stdint.h>

namespace {
typedef unsigned u32;
typedef unsigned long long u64;
typedef u32 v4u __attribute__((ext_vector_type(4)));
typedef u32 v2u __attribute__((ext_vector_type(2)));
typedef float f4 __attribute__((ext_vector_type(4)));
typedef __bf16 bf8 __attribute__((ext_vector_type(8)));
typedef __bf16 bf2 __attribute__((ext_vector_type(2)));
typedef short s4 __attribute__((ext_vector_type(4)));
#define LAS __attribute__((address_space(3)))
#define GAS __attribute__((address_space(1)))
typedef LAS unsigned char* lptr;
typedef __amdgpu_buffer_rsrc_t rsrc_t;

constexpr int DM = 1024, SK = 4096, FF = 3584;
constexpr u32 SENT = 0xFFFFFFFFu;
constexpr int SC1 = 16;
constexpr u32 SPIN_CAP = 1u << 17;

constexpr u32 WS_E = 0;
constexpr u32 WS_FT = 1024;
constexpr u32 WS_FQ = 2048;
constexpr u32 WS_FM = 3072;
constexpr u32 WS_X = 8192, X_BYTES = 16384;
constexpr u32 WS_Q = WS_X + 3 * X_BYTES, Q_BYTES = 65536;
constexpr u32 WS_A = WS_Q + 2 * Q_BYTES, A_ROW = 640, A_BYTES = 32 * 8 * 4 * A_ROW;
constexpr u32 WS_P = WS_A + 2 * A_BYTES, P_BYTES = 128 * 32 * 128;
constexpr u32 WS_M = WS_P + 2 * P_BYTES, M_BYTES = 128 * 56 * 128;
constexpr u32 WS_END = WS_M + 2 * M_BYTES;
constexpr u32 WS_ST = WS_END;

constexpr u32 L_WK = 131072;
constexpr u32 L_RED = L_WK, L_STAT = L_WK + 16384, L_IMG = L_WK + 17408;
constexpr u32 L_QIMG = L_WK, L_MX = L_WK + 2560, L_LS = L_WK + 2816, L_PIMG = L_WK + 3072;
constexpr u32 L_STAMP = L_WK + 20480;
constexpr u32 LDS_BYTES = L_WK + 20480 + 512;

#define D_Q0 0
#define DLY_Q 0
#define DLY_A 0
#define DLY_P 0
#define DLY_X 0
#define DLY_M 0
#define DLY_X2 0

#define STAMP(i) do { } while (0)

__device__ __forceinline__ void bar() { asm volatile("s_waitcnt lgkmcnt(0)\n\ts_barrier" ::: "memory"); }
template <int N> __device__ __forceinline__ void vmwait() { asm volatile("s_waitcnt vmcnt(%0)" ::"i"(N) : "memory"); }
template <int N> __device__ __forceinline__ void sleep64() { if constexpr (N > 0) __builtin_amdgcn_s_sleep(N); }

__device__ __forceinline__ void dma16(u32 voff, const void* sbase, u32 dst) {
  u32 keep;
  asm volatile("s_mov_b32 %0, m0\n\ts_mov_b32 m0, %3\n\ts_nop 2\n\tglobal_load_lds_dwordx4 %1, %2 nt\n\ts_mov_b32 m0, %0"
               : "=&s"(keep) : "v"(voff), "s"(sbase), "s"(dst) : "memory");
}
__device__ __forceinline__ f4 mfma(v4u a, v4u b, f4 c) {
  return __builtin_amdgcn_mfma_f32_16x16x32_bf16(__builtin_bit_cast(bf8, a), __builtin_bit_cast(bf8, b), c, 0, 0, 0);
}
__device__ __forceinline__ v2u trread(lptr p) { return __builtin_bit_cast(v2u, __builtin_amdgcn_ds_read_tr16_b64_v4i16((LAS s4*)p)); }
__device__ __forceinline__ u32 pk2(float a, float b) { bf2 v = {(__bf16)a, (__bf16)b}; return __builtin_bit_cast(u32, v); }
__device__ __forceinline__ u32 nosent(u32 v) { return v < 0xFFFFFFFEu ? v : 0xFFFFFFFEu; }
__device__ __forceinline__ float bflo(u32 v) { return __uint_as_float(v << 16); }
__device__ __forceinline__ float bfhi(u32 v) { return __uint_as_float(v & 0xFFFF0000u); }
__device__ __forceinline__ u32 ror8u(u32 v) { return (u32)__builtin_amdgcn_mov_dpp((int)v, 0x128, 0xf, 0xf, true); }
__device__ __forceinline__ float ror8(float v) { return __uint_as_float(ror8u(__float_as_uint(v))); }
template <int C> __device__ __forceinline__ float quadb(float v) { return __uint_as_float((u32)__builtin_amdgcn_mov_dpp((int)__float_as_uint(v), C * 0x55, 0xf, 0xf, true)); }
__device__ __forceinline__ float x16max(float x) { auto s = __builtin_amdgcn_permlane16_swap(__float_as_uint(x), __float_as_uint(x), false, false); return fmaxf(__uint_as_float(s[0]), __uint_as_float(s[1])); }
__device__ __forceinline__ float x32max(float x) { auto s = __builtin_amdgcn_permlane32_swap(__float_as_uint(x), __float_as_uint(x), false, false); return fmaxf(__uint_as_float(s[0]), __uint_as_float(s[1])); }
__device__ __forceinline__ float x16sum(float x) { auto s = __builtin_amdgcn_permlane16_swap(__float_as_uint(x), __float_as_uint(x), false, false); return __uint_as_float(s[0]) + __uint_as_float(s[1]); }
__device__ __forceinline__ float x32sum(float x) { auto s = __builtin_amdgcn_permlane32_swap(__float_as_uint(x), __float_as_uint(x), false, false); return __uint_as_float(s[0]) + __uint_as_float(s[1]); }
__device__ __forceinline__ f4 sel4(bool c, f4 t, f4 f) { f4 r; r.x = c ? t.x : f.x; r.y = c ? t.y : f.y; r.z = c ? t.z : f.z; r.w = c ? t.w : f.w; return r; }
__device__ __forceinline__ u32 max4(v4u v) { u32 a = v.x > v.y ? v.x : v.y, b = v.z > v.w ? v.z : v.w; return a > b ? a : b; }
__device__ __forceinline__ u32 umax(u32 a, u32 b) { return a > b ? a : b; }
__device__ __forceinline__ v4u ld128(rsrc_t rs, u32 off) { return __builtin_amdgcn_raw_buffer_load_b128(rs, off, 0, SC1); }
__device__ __forceinline__ v2u ld64(rsrc_t rs, u32 off) { return __builtin_amdgcn_raw_buffer_load_b64(rs, off, 0, SC1); }
__device__ __forceinline__ u32 ld32(rsrc_t rs, u32 off) { return __builtin_amdgcn_raw_buffer_load_b32(rs, off, 0, SC1); }
__device__ __forceinline__ void st128(rsrc_t rs, u32 off, v4u v) { __builtin_amdgcn_raw_buffer_store_b128(v, rs, off, 0, SC1); }
__device__ __forceinline__ void st64(rsrc_t rs, u32 off, v2u v) { __builtin_amdgcn_raw_buffer_store_b64(v, rs, off, 0, SC1); }
__device__ __forceinline__ void st32(rsrc_t rs, u32 off, u32 v) { __builtin_amdgcn_raw_buffer_store_b32(v, rs, off, 0, SC1); }

__device__ __forceinline__ u64 epoch(const unsigned char* ws) {
  u64 e;
  asm volatile("s_load_dwordx2 %0, %1, 0x0\n\ts_waitcnt lgkmcnt(0)" : "=&s"(e) : "s"(ws) : "memory");
  return e;
}
template <int N> __device__ __forceinline__ void wait_flags(rsrc_t rs, u32 off, u64 tag, int lane, bool& dead) {
  const u32 lo = (u32)tag, hi = (u32)(tag >> 32);
  for (u32 spins = 0; !dead; ++spins) {
    asm volatile("" ::: "memory");
    bool ok;
    if (N > 64) { const v4u v = ld128(rs, off + lane * 16); ok = v.x == lo && v.y == hi && v.z == lo && v.w == hi; }
    else { const v2u v = ld64(rs, off + (lane < N ? lane : N - 1) * 8); ok = v.x == lo && v.y == hi; }
    if (__all(ok)) break;
    if (spins > SPIN_CAP) dead = true;
  }
}
template <int N> __device__ __forceinline__ void gather128(rsrc_t rs, u32 off, u32 stride, v4u (&v)[N], bool& dead) {
  for (u32 spins = 0;; ++spins) {
    asm volatile("" ::: "memory");
#pragma unroll
    for (int i = 0; i < N; ++i) v[i] = ld128(rs, off + i * stride);
    u32 m = max4(v[0]);
#pragma unroll
    for (int i = 1; i < N; ++i) m = umax(m, max4(v[i]));
    if (__all(m != SENT) || dead) break;
    if (spins > SPIN_CAP) dead = true;
  }
}

__device__ __forceinline__ void slab64_issue(const unsigned char* gbase, u32 rowb, u32 ldsw, int lane) {
  const u32 voff = (u32)(lane >> 3) * rowb + (u32)(((lane & 7) ^ (((lane >> 4) & 1) << 2)) * 16);
#pragma unroll
  for (int i = 0; i < 16; ++i) dma16(voff, gbase + (size_t)i * 8 * rowb, ldsw + i * 1024);
}
template <int S> __device__ __forceinline__ void slab64_read1(lptr Lw, int lane, v4u (&A)[16]) {
  const int gq = lane >> 4, q = (lane >> 2) & 3, p = lane & 3, t = q >> 1;
  lptr a0 = Lw + (8 * gq + q) * 128 + (4 * t + p) * 16, a1 = Lw + (8 * gq + q) * 128 + (4 * (1 - t) + p) * 16;
#pragma unroll
  for (int u = 0; u < 2; ++u)
#pragma unroll
    for (int hf = 0; hf < 2; ++hf) {
      lptr a = (u ? a1 : a0) + S * 4096 + 8 * hf;
      const v2u lo = trread(a), hi = trread(a + 512);
      A[(u * 2 + hf) * 4 + S] = v4u{lo.x, lo.y, hi.x, hi.y};
    }
}
__device__ __forceinline__ void slab64_read(lptr Lw, int lane, v4u (&A)[16]) {
  vmwait<12>(); slab64_read1<0>(Lw, lane, A);
  vmwait<8>();  slab64_read1<1>(Lw, lane, A);
  vmwait<4>();  slab64_read1<2>(Lw, lane, A);
  vmwait<0>();  slab64_read1<3>(Lw, lane, A);
}

__device__ __forceinline__ void lin_partial(lptr L, const v4u (&A)[16], v4u X0, v4u X1, int lane, int w) {
  const v4u R0 = {ror8u(X0.x), ror8u(X0.y), ror8u(X0.z), ror8u(X0.w)}, R1 = {ror8u(X1.x), ror8u(X1.y), ror8u(X1.z), ror8u(X1.w)};
  const f4 z = {0.f, 0.f, 0.f, 0.f};
  const v4u ones = {0x3F803F80u, 0x3F803F80u, 0x3F803F80u, 0x3F803F80u};
  f4 G = mfma(X0, X0, z), S = mfma(ones, X0, z);
  G = mfma(X1, X1, G); S = mfma(ones, X1, S);
  f4 acc[4];
#pragma unroll
  for (int u = 0; u < 2; ++u) {
    f4 a = mfma(A[(u * 2) * 4 + 0], X0, z), c = mfma(A[(u * 2 + 1) * 4 + 0], R0, z);
    a = mfma(A[(u * 2) * 4 + 1], R0, a); c = mfma(A[(u * 2 + 1) * 4 + 1], X0, c);
    a = mfma(A[(u * 2) * 4 + 2], X1, a); c = mfma(A[(u * 2 + 1) * 4 + 2], R1, c);
    a = mfma(A[(u * 2) * 4 + 3], R1, a); c = mfma(A[(u * 2 + 1) * 4 + 3], X1, c);
    acc[u * 2] = a; acc[u * 2 + 1] = c;
  }
  const bool half = (lane >> 3) & 1;
#pragma unroll
  for (int u = 0; u < 2; ++u) *(LAS f4*)(L + L_RED + (w * 2 + u) * 1024 + lane * 16) = sel4(half, acc[u * 2 + 1], acc[u * 2]);
  const int n = lane & 15, r = n & 3;
  const float g = r == 0 ? G.x : r == 1 ? G.y : r == 2 ? G.z : G.w;
  if ((lane >> 4) == (n >> 2)) *(LAS v2u*)(L + L_STAT + n * 64 + w * 8) = v2u{__float_as_uint(S.x), __float_as_uint(g)};
}
__device__ __forceinline__ void lin_colsum_partial(lptr L, const v4u (&A)[16], int lane, int w) {
  const f4 z = {0.f, 0.f, 0.f, 0.f};
  const v4u ones = {0x3F803F80u, 0x3F803F80u, 0x3F803F80u, 0x3F803F80u};
  const bool half = (lane >> 3) & 1;
#pragma unroll
  for (int u = 0; u < 2; ++u) {
    f4 a = z, c = z;
#pragma unroll
    for (int s = 0; s < 4; ++s) { a = mfma(A[(u * 2) * 4 + s], ones, a); c = mfma(A[(u * 2 + 1) * 4 + s], ones, c); }
    *(LAS f4*)(L + L_RED + (w * 2 + u) * 1024 + lane * 16) = sel4(half, c, a);
  }
}
__device__ __forceinline__ f4 lin_sum(lptr L, int lane, int u) {
  f4 v[8];
#pragma unroll
  for (int i = 0; i < 8; ++i) v[i] = *(LAS f4*)(L + L_RED + (i * 2 + u) * 1024 + lane * 16);
  return ((v[0] + v[1]) + (v[2] + v[3])) + ((v[4] + v[5]) + (v[6] + v[7]));
}
__device__ __forceinline__ void lin_stats(lptr L, int lane, float& mean, float& var) {
  f4 v[4];
#pragma unroll
  for (int i = 0; i < 4; ++i) v[i] = *(LAS f4*)(L + L_STAT + (lane & 15) * 64 + i * 16);
  float s1 = (v[0].x + v[0].z) + (v[1].x + v[1].z) + ((v[2].x + v[2].z) + (v[3].x + v[3].z));
  float s2 = (v[0].y + v[0].w) + (v[1].y + v[1].w) + ((v[2].y + v[2].w) + (v[3].y + v[3].w));
  s1 += ror8(s1); s2 += ror8(s2);
  mean = s1 * (1.0f / DM);
  var = fmaxf(s2 * (1.0f / DM) - mean * mean, 0.f);
}

struct Args {
  const unsigned char *x, *kc, *vc, *wq, *wo, *w1, *b1, *w2, *b2;
  unsigned char *out, *ws;
};

__global__ void __launch_bounds__(512, 2) fused_decode(Args a) {
  extern __shared__ __attribute__((aligned(16))) unsigned char smem[];
  lptr L = (lptr)smem;
  const u32 lds0 = (u32)(uintptr_t)L;
  const int tid = threadIdx.x, lane = tid & 63, w = __builtin_amdgcn_readfirstlane(tid >> 6);
  const int c = blockIdx.x, xcd = c & 7, r = c >> 3;
  if (r == 31) return;
  STAMP(0);
  const rsrc_t rs = __builtin_amdgcn_make_buffer_rsrc((void*)a.ws, 0, WS_END, 0x00020000);
  const v4u sent4 = {SENT, SENT, SENT, SENT};
  const int gq = lane >> 4, half = (lane >> 3) & 1, b8 = lane & 7;
  bool dead = false;

  if (r < 16) {
    const int b = xcd, g = r >> 2, sp = r & 3, j = xcd * 16 + r;
    const u32 colb = (u32)(8 * j + 4 * (gq & 1)) * 2;
    const v2u x0 = *(const GAS v2u*)(a.x + b8 * (DM * 2) + colb), bb2 = *(const GAS v2u*)(a.b2 + colb);
    const u32 arow = (u32)(((g * 8 + b8) * 8 + b) * 4 + sp) * A_ROW;
#pragma unroll
    for (int l = 0; l < 2; ++l) {
      if (!half) st128(rs, WS_A + l * A_BYTES + arow + (16 * w + 4 * gq) * 4, sent4);
      if (w == 0 && lane < 8) st32(rs, WS_A + l * A_BYTES + arow + 512, SENT);
    }
    if (w == 0 && lane < 24) st128(rs, WS_X + (lane >> 3) * X_BYTES + j * 128 + b8 * 16, sent4);
    const size_t kvrow = (size_t)((b * 4 + g) * SK + (SK - 1024) + 256 * sp + 32 * w) * 256;
    {
      const u32 rr = lane >> 4, cs = lane & 15;
      const u32 vk = rr * 256 + ((cs ^ rr) * 16), vv = rr * 256 + ((cs ^ (rr << 1)) * 16);
#pragma unroll
      for (int i = 0; i < 8; ++i) dma16(vk ^ ((i & 3) << 6), a.kc + kvrow + i * 1024, lds0 + w * 8192 + i * 1024);
#pragma unroll
      for (int i = 0; i < 8; ++i) dma16(vv ^ ((i & 1) << 7), a.vc + kvrow + i * 1024, lds0 + 65536 + w * 8192 + i * 1024);
    }
    const u64 tag = epoch(a.ws) + 1;
    vmwait<16>();
    bar();
    if (tid == 0) st64(rs, WS_FT + j * 8, v2u{(u32)tag, (u32)(tag >> 32)});
    STAMP(1);
    v4u Kf[8], Vf[8];
    vmwait<8>();
#pragma unroll
    for (int tk = 0; tk < 2; ++tk)
#pragma unroll
      for (int s = 0; s < 4; ++s)
        Kf[tk * 4 + s] = *(LAS v4u*)(L + w * 8192 + tk * 4096 + (lane & 15) * 256 + (((gq ^ (lane & 15)) ^ (4 * s)) * 16));
    vmwait<0>();
    bar();
    {
      const int q = (lane >> 2) & 3, p = lane & 3;
      lptr vb = L + 65536 + (4 * gq + q) * 256 + (((2 * w + (p >> 1)) ^ ((4 * (gq & 1) + q) << 1)) * 16) + 8 * (p & 1);
#pragma unroll
      for (int ks = 0; ks < 8; ++ks) {
        const v2u lo = trread(vb + ks * 8192), hi = trread(vb + ks * 8192 + 4096);
        Vf[ks] = v4u{lo.x, lo.y, hi.x, hi.y};
      }
    }
    STAMP(2);
    wait_flags<64>(rs, WS_FQ, tag, lane, dead);
    STAMP(9);
    if (w == 0) wait_flags<56>(rs, WS_FM, tag, lane, dead);
    STAMP(10);
    f4 xres = {bflo(x0.x), bfhi(x0.x), bflo(x0.y), bfhi(x0.y)};
    const f4 bias2 = {bflo(bb2.x), bfhi(bb2.x), bflo(bb2.y), bfhi(bb2.y)};
    v4u sel;
    {
      const int n = lane & 15;
      sel.x = n == 0 ? 0x00003F80u : n == 1 ? 0x3F800000u : 0u;
      sel.y = n == 2 ? 0x00003F80u : n == 3 ? 0x3F800000u : 0u;
      sel.z = n == 4 ? 0x00003F80u : n == 5 ? 0x3F800000u : 0u;
      sel.w = n == 6 ? 0x00003F80u : n == 7 ? 0x3F800000u : 0u;
    }
    const f4 z = {0.f, 0.f, 0.f, 0.f};
#pragma unroll 1
    for (int l = 0; l < 2; ++l) {
      if (l) sleep64<DLY_Q>(); else sleep64<D_Q0>();
      v4u qv[1];
      gather128<1>(rs, WS_Q + l * Q_BYTES + b * 8192 + g * 2048 + w * 256 + (lane & 15) * 16, 0, qv, dead);
      STAMP(3 + l * 8);
      if (lane < 16) *(LAS v4u*)(L + L_QIMG + w * 288 + lane * 16) = qv[0];
      bar();
      f4 s0 = z, s1 = z;
#pragma unroll
      for (int s = 0; s < 4; ++s) {
        const v4u qf = *(LAS v4u*)(L + L_QIMG + b8 * 288 + s * 64 + gq * 16);
        s0 = mfma(Kf[s], qf, s0); s1 = mfma(Kf[4 + s], qf, s1);
      }
      constexpr float SC = 0.08838834764831845f * 1.4426950408889634f;
      const f4 sc = sel4(half, s1, s0) * SC;
      float mx = fmaxf(fmaxf(sc.x, sc.y), fmaxf(sc.z, sc.w));
      mx = fmaxf(mx, ror8(mx)); mx = x16max(mx); mx = x32max(mx);
      if (lane < 8) *(LAS float*)(L + L_MX + lane * 32 + w * 4) = mx;
      bar();
      {
        const f4 m0 = *(LAS f4*)(L + L_MX + b8 * 32), m1 = *(LAS f4*)(L + L_MX + b8 * 32 + 16);
        mx = fmaxf(fmaxf(fmaxf(m0.x, m0.y), fmaxf(m0.z, m0.w)), fmaxf(fmaxf(m1.x, m1.y), fmaxf(m1.z, m1.w)));
      }
      const float p0 = __builtin_amdgcn_exp2f(sc.x - mx), p1 = __builtin_amdgcn_exp2f(sc.y - mx), p2 = __builtin_amdgcn_exp2f(sc.z - mx), p3 = __builtin_amdgcn_exp2f(sc.w - mx);
      *(LAS v2u*)(L + L_PIMG + ((w * 4 + gq) * 8 + b8) * 16 + half * 8) = v2u{pk2(p0, p1), pk2(p2, p3)};
      float ls = (p0 + p1) + (p2 + p3);
      ls += ror8(ls); ls = x16sum(ls); ls = x32sum(ls);
      if (lane < 8) *(LAS float*)(L + L_LS + lane * 32 + w * 4) = ls;
      bar();
      f4 o0 = z, o1 = z;
#pragma unroll
      for (int ks = 0; ks < 8; ks += 2) {
        const v4u pa = *(LAS v4u*)(L + L_PIMG + ((ks * 4 + gq) * 8 + b8) * 16), pb = *(LAS v4u*)(L + L_PIMG + (((ks + 1) * 4 + gq) * 8 + b8) * 16);
        o0 = mfma(Vf[ks], pa, o0); o1 = mfma(Vf[ks + 1], pb, o1);
      }
      {
        const f4 l0 = *(LAS f4*)(L + L_LS + b8 * 32), l1 = *(LAS f4*)(L + L_LS + b8 * 32 + 16);
        const float tot = ((l0.x + l0.y) + (l0.z + l0.w)) + ((l1.x + l1.y) + (l1.z + l1.w));
        const f4 o = (o0 + o1) * __builtin_amdgcn_rcpf(tot);
        if (!half) st128(rs, WS_A + l * A_BYTES + arow + (16 * w + 4 * gq) * 4,
                         v4u{nosent(__float_as_uint(o.x)), nosent(__float_as_uint(o.y)), nosent(__float_as_uint(o.z)), nosent(__float_as_uint(o.w))});
        if (w == 0 && lane < 8) st32(rs, WS_A + l * A_BYTES + arow + 512, nosent(__float_as_uint(mx + __builtin_amdgcn_logf(tot))));
      }
      STAMP(4 + l * 8);
      if (w == 0) {
        {
          sleep64<DLY_P>();
          v4u pv[4];
          gather128<4>(rs, WS_P + l * P_BYTES + j * 4096 + lane * 16, 1024, pv, dead);
          STAMP(5 + l * 8);
          f4 acc = z;
#pragma unroll
          for (int i = 0; i < 4; ++i) acc = mfma(sel, pv[i], acc);
          xres.x += acc.x + ror8(acc.x); xres.y += acc.y + ror8(acc.y); xres.z += acc.z + ror8(acc.z); xres.w += acc.w + ror8(acc.w);
          if (lane < 32 && !half) st64(rs, WS_X + (2 * l) * X_BYTES + j * 128 + b8 * 16 + gq * 8, v2u{nosent(pk2(xres.x, xres.y)), nosent(pk2(xres.z, xres.w))});
          STAMP(6 + l * 8);
        }
        {
          sleep64<DLY_M>();
          v4u pv[7];
          gather128<7>(rs, WS_M + l * M_BYTES + j * 7168 + lane * 16, 1024, pv, dead);
          STAMP(7 + l * 8);
          f4 acc = z;
#pragma unroll
          for (int i = 0; i < 7; ++i) acc = mfma(sel, pv[i], acc);
          xres.x += (acc.x + ror8(acc.x)) + bias2.x; xres.y += (acc.y + ror8(acc.y)) + bias2.y;
          xres.z += (acc.z + ror8(acc.z)) + bias2.z; xres.w += (acc.w + ror8(acc.w)) + bias2.w;
          const v2u o = {pk2(xres.x, xres.y), pk2(xres.z, xres.w)};
          if (lane < 32 && !half) {
            if (l == 0) st64(rs, WS_X + X_BYTES + j * 128 + b8 * 16 + gq * 8, v2u{nosent(o.x), nosent(o.y)});
            else *(GAS v2u*)(a.out + b8 * (DM * 2) + (8 * j + 4 * gq) * 2) = o;
          }
          STAMP(8 + l * 8);
        }
      }
      bar();
    }
    if (c == 0 && tid == 0) st64(rs, WS_E, v2u{(u32)tag, (u32)(tag >> 32)});
  } else if (r < 24) {
    const int qi = xcd * 8 + (r - 16), h = qi >> 1, ch = qi & 1;
    const v4u x00 = *(const GAS v4u*)(a.x + b8 * (DM * 2) + (128 * w + 32 * half + 8 * gq) * 2);
    const v4u x01 = *(const GAS v4u*)(a.x + b8 * (DM * 2) + (128 * w + 64 + 32 * half + 8 * gq) * 2);
    const u32 qoff = (u32)b8 * 8192 + (64 * qi + 32 * (w & 1) + 8 * gq) * 2;
    const u32 poff = (u32)((64 * ch + 8 * w + 4 * half + gq) * 32 + h) * 128 + b8 * 16;
#pragma unroll
    for (int l = 0; l < 2; ++l) {
      st128(rs, WS_P + l * P_BYTES + poff, sent4);
      if (w < 2 && !half) st128(rs, WS_Q + l * Q_BYTES + qoff, sent4);
    }
    slab64_issue(a.wq + (size_t)(128 * w) * 8192 + qi * 128, 8192, lds0 + w * 16384, lane);
    const u64 tag = epoch(a.ws) + 1;
    vmwait<16>();
    bar();
    if (tid == 0) st64(rs, WS_FQ + qi * 8, v2u{(u32)tag, (u32)(tag >> 32)});
    STAMP(1);
    v4u Wq[16], Wo[16];
    slab64_read(L + w * 16384, lane, Wq);
    STAMP(2);
    lin_colsum_partial(L, Wq, lane, w);
    bar();
    f4 cs = {0.f, 0.f, 0.f, 0.f};
    if (w < 2) cs = lin_sum(L, lane, w);
    bar();
    v4u X0 = x00, X1 = x01;
#pragma unroll 1
    for (int l = 0; l < 2; ++l) {
      if (l) {
        sleep64<DLY_X2>();
        v4u xv[2];
        gather128<2>(rs, WS_X + X_BYTES + (16 * w + 4 * half + gq) * 128 + b8 * 16, 1024, xv, dead);
        X0 = xv[0]; X1 = xv[1];
      }
      STAMP(3 + l * 8);
      lin_partial(L, Wq, X0, X1, lane, w);
      bar();
      if (w < 2) {
        const f4 acc = lin_sum(L, lane, w);
        float mean, var;
        lin_stats(L, lane, mean, var);
        const float rstd = __builtin_amdgcn_rsqf(var + 1e-5f);
        const f4 qv = (acc - mean * cs) * rstd;
        const u32 d0 = nosent(pk2(qv.x, qv.y)), d1 = nosent(pk2(qv.z, qv.w));
        const u32 e0 = ror8u(d0), e1 = ror8u(d1);
        if (!half) st128(rs, WS_Q + l * Q_BYTES + qoff, v4u{d0, d1, e0, e1});
      }
      STAMP(4 + l * 8);
      if (l == 0) {
        bar();
        slab64_issue(a.wo + (size_t)(128 * h) * 2048 + (512 * ch + 64 * w) * 2, 2048, lds0 + w * 16384, lane);
        slab64_read(L + w * 16384, lane, Wo);
        wait_flags<128>(rs, WS_FT, tag, lane, dead);
      }
      STAMP(5 + l * 8);
      sleep64<DLY_A>();
      {
        const u32 row = WS_A + l * A_BYTES + (u32)((h * 8 + w) * 4) * A_ROW;
        v2u ov[4]; u32 le;
        for (u32 spins = 0;; ++spins) {
          asm volatile("" ::: "memory");
#pragma unroll
          for (int s = 0; s < 4; ++s) ov[s] = ld64(rs, row + s * A_ROW + lane * 8);
          le = ld32(rs, row + (lane & 3) * A_ROW + 512);
          u32 m = le;
#pragma unroll
          for (int s = 0; s < 4; ++s) m = umax(m, umax(ov[s].x, ov[s].y));
          if (__all(m != SENT) || dead) break;
          if (spins > SPIN_CAP) dead = true;
        }
        STAMP(6 + l * 8);
        const float lf = __uint_as_float(le);
        const float e0 = quadb<0>(lf), e1 = quadb<1>(lf), e2 = quadb<2>(lf), e3 = quadb<3>(lf);
        const float mx = fmaxf(fmaxf(e0, e1), fmaxf(e2, e3));
        const float w0 = __builtin_amdgcn_exp2f(e0 - mx), w1 = __builtin_amdgcn_exp2f(e1 - mx), w2 = __builtin_amdgcn_exp2f(e2 - mx), w3 = __builtin_amdgcn_exp2f(e3 - mx);
        const float inv = __builtin_amdgcn_rcpf((w0 + w1) + (w2 + w3));
        const float oa = (w0 * __uint_as_float(ov[0].x) + w1 * __uint_as_float(ov[1].x)) + (w2 * __uint_as_float(ov[2].x) + w3 * __uint_as_float(ov[3].x));
        const float ob = (w0 * __uint_as_float(ov[0].y) + w1 * __uint_as_float(ov[1].y)) + (w2 * __uint_as_float(ov[2].y) + w3 * __uint_as_float(ov[3].y));
        *(LAS u32*)(L + L_IMG + w * 288 + lane * 4) = pk2(oa * inv, ob * inv);
      }
      bar();
      {
        const f4 z = {0.f, 0.f, 0.f, 0.f};
        f4 acc[4] = {z, z, z, z};
#pragma unroll
        for (int s = 0; s < 4; ++s) {
          const v4u of = *(LAS v4u*)(L + L_IMG + b8 * 288 + s * 64 + gq * 16);
#pragma unroll
          for (int t = 0; t < 4; ++t) acc[t] = mfma(Wo[t * 4 + s], of, acc[t]);
        }
        const f4 lo = sel4(half, acc[2], acc[0]), hi = sel4(half, acc[3], acc[1]);
        st128(rs, WS_P + l * P_BYTES + poff, v4u{nosent(pk2(lo.x, lo.y)), nosent(pk2(lo.z, lo.w)), nosent(pk2(hi.x, hi.y)), nosent(pk2(hi.z, hi.w))});
      }
      STAMP(7 + l * 8);
    }
  } else {
    const int mi = xcd * 7 + (r - 24);
    const v2u bb1 = *(const GAS v2u*)(a.b1 + (64 * mi + 32 * (w & 1) + 8 * gq + 4 * half) * 2);
    const u32 moff = (u32)((16 * w + 4 * half + gq) * 56 + mi) * 128 + b8 * 16;
#pragma unroll
    for (int l = 0; l < 2; ++l) {
      st128(rs, WS_M + l * M_BYTES + moff, sent4);
      st128(rs, WS_M + l * M_BYTES + moff + 8 * 56 * 128, sent4);
    }
    slab64_issue(a.w1 + (size_t)(128 * w) * (FF * 2) + mi * 128, FF * 2, lds0 + w * 16384, lane);
    const u64 tag = epoch(a.ws) + 1;
    vmwait<16>();
    bar();
    if (tid == 0) st64(rs, WS_FM + mi * 8, v2u{(u32)tag, (u32)(tag >> 32)});
    STAMP(1);
    v4u W1[16], W2[16];
    slab64_read(L + w * 16384, lane, W1);
    asm volatile("s_waitcnt lgkmcnt(0)" ::: "memory");
    STAMP(2);
    {
      const u32 rr = lane >> 4, cs = lane & 15;
      const u32 voff = rr * 2048 + ((cs ^ (rr << 2)) * 16);
#pragma unroll
      for (int i = 0; i < 16; ++i) dma16(voff, a.w2 + (size_t)(64 * mi + 4 * i) * 2048 + w * 256, lds0 + w * 16384 + i * 1024);
    }
    lin_colsum_partial(L, W1, lane, w);
    bar();
    f4 cs = {0.f, 0.f, 0.f, 0.f};
    if (w < 2) cs = lin_sum(L, lane, w);
    {
      const int q = (lane >> 2) & 3, p = lane & 3;
#pragma unroll
      for (int s = 0; s < 2; ++s) {
        if (s == 0) vmwait<8>(); else vmwait<0>();
#pragma unroll
        for (int u = 0; u < 4; ++u)
#pragma unroll
          for (int hf = 0; hf < 2; ++hf) {
            lptr ad = L + w * 16384 + (32 * s + 8 * gq + q) * 256 + (4 * (u ^ q) + p) * 16 + 8 * hf;
            const v2u lo = trread(ad), hi = trread(ad + 1024);
            W2[(u * 2 + hf) * 2 + s] = v4u{lo.x, lo.y, hi.x, hi.y};
          }
      }
    }
    STAMP(3);
    wait_flags<128>(rs, WS_FT, tag, lane, dead);
    const f4 bias1 = {bflo(bb1.x), bfhi(bb1.x), bflo(bb1.y), bfhi(bb1.y)};
    bar();
#pragma unroll 1
    for (int l = 0; l < 2; ++l) {
      sleep64<DLY_X>();
      v4u xv[2];
      gather128<2>(rs, WS_X + (2 * l) * X_BYTES + (16 * w + 4 * half + gq) * 128 + b8 * 16, 1024, xv, dead);
      STAMP(4 + l * 8);
      lin_partial(L, W1, xv[0], xv[1], lane, w);
      bar();
      if (w < 2) {
        const f4 acc = lin_sum(L, lane, w);
        float mean, var;
        lin_stats(L, lane, mean, var);
        const float r1 = __builtin_amdgcn_rsqf(var + 1e-5f);
        const float r2 = __builtin_amdgcn_rsqf(var * r1 * r1 + 1e-5f);
        const f4 zz = (acc - mean * cs) * (r1 * r2) + bias1;
        float hv[4];
#pragma unroll
        for (int i = 0; i < 4; ++i) {
          const float t = zz[i];
          const float uu = t * (1.0f + 0.044715f * t * t) * (-2.0f * 0.7978845608028654f * 1.4426950408889634f);
          hv[i] = t * __builtin_amdgcn_rcpf(1.0f + __builtin_amdgcn_exp2f(uu));
        }
        *(LAS v2u*)(L + L_IMG + b8 * 160 + (32 * w + 8 * gq + 4 * half) * 2) = v2u{pk2(hv[0], hv[1]), pk2(hv[2], hv[3])};
      }
      bar();
      STAMP(5 + l * 8);
      {
        const f4 z = {0.f, 0.f, 0.f, 0.f};
        const v4u h0 = *(LAS v4u*)(L + L_IMG + b8 * 160 + gq * 16), h1 = *(LAS v4u*)(L + L_IMG + b8 * 160 + 64 + gq * 16);
#pragma unroll
        for (int uu = 0; uu < 2; ++uu) {
          f4 acc[4];
#pragma unroll
          for (int t = 0; t < 4; ++t) { acc[t] = mfma(W2[(uu * 4 + t) * 2], h0, z); acc[t] = mfma(W2[(uu * 4 + t) * 2 + 1], h1, acc[t]); }
          const f4 lo = sel4(half, acc[2], acc[0]), hi = sel4(half, acc[3], acc[1]);
          st128(rs, WS_M + l * M_BYTES + moff + uu * (8 * 56 * 128),
                v4u{nosent(pk2(lo.x, lo.y)), nosent(pk2(lo.z, lo.w)), nosent(pk2(hi.x, hi.y)), nosent(pk2(hi.z, hi.w))});
        }
      }
      STAMP(6 + l * 8);
    }
  }
}
}

extern "C" void kernel_launch(void* const* d_in, const int* in_sizes, int n_in, void* d_out, int out_size, void* d_ws, size_t ws_size,
                              hipStream_t stream) {
  Args a;
  a.x = (const unsigned char*)d_in[0];
  a.kc = (const unsigned char*)d_in[1];
  a.vc = (const unsigned char*)d_in[2];
  a.wq = (const unsigned char*)d_in[3];
  a.wo = (const unsigned char*)d_in[4];
  a.w1 = (const unsigned char*)d_in[6];
  a.b1 = (const unsigned char*)d_in[7];
  a.w2 = (const unsigned char*)d_in[8];
  a.b2 = (const unsigned char*)d_in[9];
  a.out = (unsigned char*)d_out;
  a.ws = (unsigned char*)d_ws;
  (void)hipFuncSetAttribute((const void*)fused_decode, hipFuncAttributeMaxDynamicSharedMemorySize, LDS_BYTES);
  hipLaunchKernelGGL(fused_decode, dim3(256), dim3(512), LDS_BYTES, stream, a);
}
```

```cpp
#include <hip/hip_runtime.h>
#include <stdint.h>
#include <stdio.h>

namespace {
typedef unsigned u32;
typedef unsigned long long u64;
typedef u32 v4u __attribute__((ext_vector_type(4)));
typedef u32 v2u __attribute__((ext_vector_type(2)));
typedef float f4 __attribute__((ext_vector_type(4)));
typedef __bf16 bf8 __attribute__((ext_vector_type(8)));
typedef __bf16 bf2 __attribute__((ext_vector_type(2)));
typedef short s4 __attribute__((ext_vector_type(4)));
#define LAS __attribute__((address_space(3)))
#define GAS __attribute__((address_space(1)))
typedef LAS unsigned char* lptr;
typedef __amdgpu_buffer_rsrc_t rsrc_t;

constexpr int DM = 1024, SK = 4096, FF = 3584;
constexpr u32 SENT = 0xFFFFFFFFu;
constexpr int SC1 = 16;
constexpr u32 SPIN_CAP = 1u << 17;

constexpr u32 WS_E = 0;
constexpr u32 WS_FT = 1024;
constexpr u32 WS_FQ = 2048;
constexpr u32 WS_FM = 3072;
constexpr u32 WS_X = 8192, X_BYTES = 16384;
constexpr u32 WS_Q = WS_X + 3 * X_BYTES, Q_BYTES = 65536;
constexpr u32 WS_A = WS_Q + 2 * Q_BYTES, A_ROW = 640, A_BYTES = 32 * 8 * 4 * A_ROW;
constexpr u32 WS_P = WS_A + 2 * A_BYTES, P_BYTES = 128 * 32 * 128;
constexpr u32 WS_M = WS_P + 2 * P_BYTES, M_BYTES = 128 * 56 * 128;
constexpr u32 WS_END = WS_M + 2 * M_BYTES;
constexpr u32 WS_ST = WS_END;

constexpr u32 L_WK = 131072;
constexpr u32 L_RED = L_WK, L_STAT = L_WK + 16384, L_IMG = L_WK + 17408;
constexpr u32 L_QIMG = L_WK, L_MX = L_WK + 2560, L_LS = L_WK + 2816, L_PIMG = L_WK + 3072;
constexpr u32 L_STAMP = L_WK + 20480;
constexpr u32 LDS_BYTES = L_WK + 20480 + 512;

#define D_Q0 0
#define DLY_Q 0
#define DLY_A 0
#define DLY_P 0
#define DLY_X 0
#define DLY_M 0
#define DLY_X2 0

constexpr int NCLR = 2;
#define STAMP(i) do { } while (0)

__device__ __forceinline__ void bar() { asm volatile("s_waitcnt lgkmcnt(0)\n\ts_barrier" ::: "memory"); }
template <int N> __device__ __forceinline__ void vmwait() { asm volatile("s_waitcnt vmcnt(%0)" ::"i"(N) : "memory"); }
template <int N> __device__ __forceinline__ void sleep64() { if constexpr (N > 0) __builtin_amdgcn_s_sleep(N); }

__device__ __forceinline__ void dma16(u32 voff, const void* sbase, u32 dst) {
  u32 keep;
  asm volatile("s_mov_b32 %0, m0\n\ts_mov_b32 m0, %3\n\ts_nop 2\n\tglobal_load_lds_dwordx4 %1, %2 nt\n\ts_mov_b32 m0, %0"
               : "=&s"(keep) : "v"(voff), "s"(sbase), "s"(dst) : "memory");
}
__device__ __forceinline__ f4 mfma(v4u a, v4u b, f4 c) {
  return __builtin_amdgcn_mfma_f32_16x16x32_bf16(__builtin_bit_cast(bf8, a), __builtin_bit_cast(bf8, b), c, 0, 0, 0);
}
__device__ __forceinline__ v2u trread(lptr p) { return __builtin_bit_cast(v2u, __builtin_amdgcn_ds_read_tr16_b64_v4i16((LAS s4*)p)); }
__device__ __forceinline__ u32 pk2(float a, float b) { bf2 v = {(__bf16)a, (__bf16)b}; return __builtin_bit_cast(u32, v); }
__device__ __forceinline__ u32 nosent(u32 v) { return v < 0xFFFFFFFEu ? v : 0xFFFFFFFEu; }
__device__ __forceinline__ float bflo(u32 v) { return __uint_as_float(v << 16); }
__device__ __forceinline__ float bfhi(u32 v) { return __uint_as_float(v & 0xFFFF0000u); }
__device__ __forceinline__ u32 ror8u(u32 v) { return (u32)__builtin_amdgcn_mov_dpp((int)v, 0x128, 0xf, 0xf, true); }
__device__ __forceinline__ float ror8(float v) { return __uint_as_float(ror8u(__float_as_uint(v))); }
template <int C> __device__ __forceinline__ float quadb(float v) { return __uint_as_float((u32)__builtin_amdgcn_mov_dpp((int)__float_as_uint(v), C * 0x55, 0xf, 0xf, true)); }
__device__ __forceinline__ float x16max(float x) { auto s = __builtin_amdgcn_permlane16_swap(__float_as_uint(x), __float_as_uint(x), false, false); return fmaxf(__uint_as_float(s[0]), __uint_as_float(s[1])); }
__device__ __forceinline__ float x32max(float x) { auto s = __builtin_amdgcn_permlane32_swap(__float_as_uint(x), __float_as_uint(x), false, false); return fmaxf(__uint_as_float(s[0]), __uint_as_float(s[1])); }
__device__ __forceinline__ float x16sum(float x) { auto s = __builtin_amdgcn_permlane16_swap(__float_as_uint(x), __float_as_uint(x), false, false); return __uint_as_float(s[0]) + __uint_as_float(s[1]); }
__device__ __forceinline__ float x32sum(float x) { auto s = __builtin_amdgcn_permlane32_swap(__float_as_uint(x), __float_as_uint(x), false, false); return __uint_as_float(s[0]) + __uint_as_float(s[1]); }
__device__ __forceinline__ f4 sel4(bool c, f4 t, f4 f) { f4 r; r.x = c ? t.x : f.x; r.y = c ? t.y : f.y; r.z = c ? t.z : f.z; r.w = c ? t.w : f.w; return r; }
__device__ __forceinline__ u32 max4(v4u v) { u32 a = v.x > v.y ? v.x : v.y, b = v.z > v.w ? v.z : v.w; return a > b ? a : b; }
__device__ __forceinline__ u32 umax(u32 a, u32 b) { return a > b ? a : b; }
__device__ __forceinline__ v4u ld128(rsrc_t rs, u32 off) { return __builtin_amdgcn_raw_buffer_load_b128(rs, off, 0, SC1); }
__device__ __forceinline__ v2u ld64(rsrc_t rs, u32 off) { return __builtin_amdgcn_raw_buffer_load_b64(rs, off, 0, SC1); }
__device__ __forceinline__ u32 ld32(rsrc_t rs, u32 off) { return __builtin_amdgcn_raw_buffer_load_b32(rs, off, 0, SC1); }
__device__ __forceinline__ void st128(rsrc_t rs, u32 off, v4u v) { __builtin_amdgcn_raw_buffer_store_b128(v, rs, off, 0, SC1); }
__device__ __forceinline__ void st64(rsrc_t rs, u32 off, v2u v) { __builtin_amdgcn_raw_buffer_store_b64(v, rs, off, 0, SC1); }
__device__ __forceinline__ void st32(rsrc_t rs, u32 off, u32 v) { __builtin_amdgcn_raw_buffer_store_b32(v, rs, off, 0, SC1); }

__device__ __forceinline__ u64 epoch(const unsigned char* ws) {
  u64 e;
  asm volatile("s_load_dwordx2 %0, %1, 0x0\n\ts_waitcnt lgkmcnt(0)" : "=&s"(e) : "s"(ws) : "memory");
  return e;
}
template <int N> __device__ __forceinline__ void wait_flags(rsrc_t rs, u32 off, u64 tag, int lane, bool& dead) {
  const u32 lo = (u32)tag, hi = (u32)(tag >> 32);
  for (u32 spins = 0; !dead; ++spins) {
    asm volatile("" ::: "memory");
    bool ok;
    if (N > 64) { const v4u v = ld128(rs, off + lane * 16); ok = v.x == lo && v.y == hi && v.z == lo && v.w == hi; }
    else { const v2u v = ld64(rs, off + (lane < N ? lane : N - 1) * 8); ok = v.x == lo && v.y == hi; }
    if (__all(ok)) break;
    if (spins > SPIN_CAP) dead = true;
  }
}
template <int N> __device__ __forceinline__ void gather128(rsrc_t rs, u32 off, u32 stride, v4u (&v)[N], bool& dead) {
  for (u32 spins = 0;; ++spins) {
    asm volatile("" ::: "memory");
#pragma unroll
    for (int i = 0; i < N; ++i) v[i] = ld128(rs, off + i * stride);
    u32 m = max4(v[0]);
#pragma unroll
    for (int i = 1; i < N; ++i) m = umax(m, max4(v[i]));
    if (__all(m != SENT) || dead) break;
    if (spins > SPIN_CAP) dead = true;
  }
}

__device__ __forceinline__ void slab64_issue(const unsigned char* gbase, u32 rowb, u32 ldsw, int lane) {
  const u32 voff = (u32)(lane >> 3) * rowb + (u32)(((lane & 7) ^ (((lane >> 4) & 1) << 2)) * 16);
#pragma unroll
  for (int i = 0; i < 16; ++i) dma16(voff, gbase + (size_t)i * 8 * rowb, ldsw + i * 1024);
}
template <int S> __device__ __forceinline__ void slab64_read1(lptr Lw, int lane, v4u (&A)[16]) {
  const int gq = lane >> 4, q = (lane >> 2) & 3, p = lane & 3, t = q >> 1;
  lptr a0 = Lw + (8 * gq + q) * 128 + (4 * t + p) * 16, a1 = Lw + (8 * gq + q) * 128 + (4 * (1 - t) + p) * 16;
#pragma unroll
  for (int u = 0; u < 2; ++u)
#pragma unroll
    for (int hf = 0; hf < 2; ++hf) {
      lptr a = (u ? a1 : a0) + S * 4096 + 8 * hf;
      const v2u lo = trread(a), hi = trread(a + 512);
      A[(u * 2 + hf) * 4 + S] = v4u{lo.x, lo.y, hi.x, hi.y};
    }
}
__device__ __forceinline__ void slab64_read(lptr Lw, int lane, v4u (&A)[16]) {
  vmwait<12>(); slab64_read1<0>(Lw, lane, A);
  vmwait<8>();  slab64_read1<1>(Lw, lane, A);
  vmwait<4>();  slab64_read1<2>(Lw, lane, A);
  vmwait<0>();  slab64_read1<3>(Lw, lane, A);
}

__device__ __forceinline__ void lin_partial(lptr L, const v4u (&A)[16], v4u X0, v4u X1, int lane, int w) {
  const v4u R0 = {ror8u(X0.x), ror8u(X0.y), ror8u(X0.z), ror8u(X0.w)}, R1 = {ror8u(X1.x), ror8u(X1.y), ror8u(X1.z), ror8u(X1.w)};
  const f4 z = {0.f, 0.f, 0.f, 0.f};
  const v4u ones = {0x3F803F80u, 0x3F803F80u, 0x3F803F80u, 0x3F803F80u};
  f4 G = mfma(X0, X0, z), S = mfma(ones, X0, z);
  G = mfma(X1, X1, G); S = mfma(ones, X1, S);
  f4 acc[4];
#pragma unroll
  for (int u = 0; u < 2; ++u) {
    f4 a = mfma(A[(u * 2) * 4 + 0], X0, z), c = mfma(A[(u * 2 + 1) * 4 + 0], R0, z);
    a = mfma(A[(u * 2) * 4 + 1], R0, a); c = mfma(A[(u * 2 + 1) * 4 + 1], X0, c);
    a = mfma(A[(u * 2) * 4 + 2], X1, a); c = mfma(A[(u * 2 + 1) * 4 + 2], R1, c);
    a = mfma(A[(u * 2) * 4 + 3], R1, a); c = mfma(A[(u * 2 + 1) * 4 + 3], X1, c);
    acc[u * 2] = a; acc[u * 2 + 1] = c;
  }
  const bool half = (lane >> 3) & 1;
#pragma unroll
  for (int u = 0; u < 2; ++u) *(LAS f4*)(L + L_RED + (w * 2 + u) * 1024 + lane * 16) = sel4(half, acc[u * 2 + 1], acc[u * 2]);
  const int n = lane & 15, r = n & 3;
  const float g = r == 0 ? G.x : r == 1 ? G.y : r == 2 ? G.z : G.w;
  if ((lane >> 4) == (n >> 2)) *(LAS v2u*)(L + L_STAT + n * 64 + w * 8) = v2u{__float_as_uint(S.x), __float_as_uint(g)};
}
__device__ __forceinline__ void lin_colsum_partial(lptr L, const v4u (&A)[16], int lane, int w) {
  const f4 z = {0.f, 0.f, 0.f, 0.f};
  const v4u ones = {0x3F803F80u, 0x3F803F80u, 0x3F803F80u, 0x3F803F80u};
  const bool half = (lane >> 3) & 1;
#pragma unroll
  for (int u = 0; u < 2; ++u) {
    f4 a = z, c = z;
#pragma unroll
    for (int s = 0; s < 4; ++s) { a = mfma(A[(u * 2) * 4 + s], ones, a); c = mfma(A[(u * 2 + 1) * 4 + s], ones, c); }
    *(LAS f4*)(L + L_RED + (w * 2 + u) * 1024 + lane * 16) = sel4(half, c, a);
  }
}
__device__ __forceinline__ f4 lin_sum(lptr L, int lane, int u) {
  f4 v[8];
#pragma unroll
  for (int i = 0; i < 8; ++i) v[i] = *(LAS f4*)(L + L_RED + (i * 2 + u) * 1024 + lane * 16);
  return ((v[0] + v[1]) + (v[2] + v[3])) + ((v[4] + v[5]) + (v[6] + v[7]));
}
__device__ __forceinline__ void lin_stats(lptr L, int lane, float& mean, float& var) {
  f4 v[4];
#pragma unroll
  for (int i = 0; i < 4; ++i) v[i] = *(LAS f4*)(L + L_STAT + (lane & 15) * 64 + i * 16);
  float s1 = (v[0].x + v[0].z) + (v[1].x + v[1].z) + ((v[2].x + v[2].z) + (v[3].x + v[3].z));
  float s2 = (v[0].y + v[0].w) + (v[1].y + v[1].w) + ((v[2].y + v[2].w) + (v[3].y + v[3].w));
  s1 += ror8(s1); s2 += ror8(s2);
  mean = s1 * (1.0f / DM);
  var = fmaxf(s2 * (1.0f / DM) - mean * mean, 0.f);
}

struct Args {
  const unsigned char *x, *kc, *vc, *wq, *wo, *w1, *b1, *w2, *b2;
  unsigned char *out, *ws;
};

__global__ void __launch_bounds__(512, 2) fused_decode(Args a) {
  extern __shared__ __attribute__((aligned(16))) unsigned char smem[];
  lptr L = (lptr)smem;
  const u32 lds0 = (u32)(uintptr_t)L;
  const int tid = threadIdx.x, lane = tid & 63, w = __builtin_amdgcn_readfirstlane(tid >> 6);
  const int c = blockIdx.x, xcd = c & 7, r = c >> 3;
  if (r == 31) return;
  STAMP(0);
  const rsrc_t rs = __builtin_amdgcn_make_buffer_rsrc((void*)a.ws, 0, WS_END, 0x00020000);
  const v4u sent4 = {SENT, SENT, SENT, SENT};
  const int gq = lane >> 4, half = (lane >> 3) & 1, b8 = lane & 7;
  bool dead = false;
  const u64 dbg_t0 = __builtin_amdgcn_s_memrealtime(), dbg_c0 = __builtin_amdgcn_s_memtime();

  if (r < 16) {
    const int b = xcd, g = r >> 2, sp = r & 3, j = xcd * 16 + r;
    const u32 colb = (u32)(8 * j + 4 * (gq & 1)) * 2;
    const v2u x0 = *(const GAS v2u*)(a.x + b8 * (DM * 2) + colb), bb2 = *(const GAS v2u*)(a.b2 + colb);
    const u32 arow = (u32)(((g * 8 + b8) * 8 + b) * 4 + sp) * A_ROW;
#pragma unroll
    for (int l = 0; l < NCLR; ++l) {
      if (!half) st128(rs, WS_A + l * A_BYTES + arow + (16 * w + 4 * gq) * 4, sent4);
      if (w == 0 && lane < 8) st32(rs, WS_A + l * A_BYTES + arow + 512, SENT);
    }
    if (NCLR && w == 0 && lane < 24) st128(rs, WS_X + (lane >> 3) * X_BYTES + j * 128 + b8 * 16, sent4);
    const size_t kvrow = (size_t)((b * 4 + g) * SK + (SK - 1024) + 256 * sp + 32 * w) * 256;
    {
      const u32 rr = lane >> 4, cs = lane & 15;
      const u32 vk = rr * 256 + ((cs ^ rr) * 16), vv = rr * 256 + ((cs ^ (rr << 1)) * 16);
#pragma unroll
      for (int i = 0; i < 8; ++i) dma16(vk ^ ((i & 3) << 6), a.kc + kvrow + i * 1024, lds0 + w * 8192 + i * 1024);
#pragma unroll
      for (int i = 0; i < 8; ++i) dma16(vv ^ ((i & 1) << 7), a.vc + kvrow + i * 1024, lds0 + 65536 + w * 8192 + i * 1024);
    }
    STAMP(17);
    const u64 tag = epoch(a.ws) + 1;
    STAMP(18);
    vmwait<16>();
    STAMP(19);
    bar();
    if (tid == 0) st64(rs, WS_FT + j * 8, v2u{(u32)tag, (u32)(tag >> 32)});
    STAMP(1);
    v4u Kf[8], Vf[8];
    vmwait<8>();
#pragma unroll
    for (int tk = 0; tk < 2; ++tk)
#pragma unroll
      for (int s = 0; s < 4; ++s)
        Kf[tk * 4 + s] = *(LAS v4u*)(L + w * 8192 + tk * 4096 + (lane & 15) * 256 + (((gq ^ (lane & 15)) ^ (4 * s)) * 16));
    vmwait<0>();
    bar();
    {
      const int q = (lane >> 2) & 3, p = lane & 3;
      lptr vb = L + 65536 + (4 * gq + q) * 256 + (((2 * w + (p >> 1)) ^ ((4 * (gq & 1) + q) << 1)) * 16) + 8 * (p & 1);
#pragma unroll
      for (int ks = 0; ks < 8; ++ks) {
        const v2u lo = trread(vb + ks * 8192), hi = trread(vb + ks * 8192 + 4096);
        Vf[ks] = v4u{lo.x, lo.y, hi.x, hi.y};
      }
    }
    STAMP(2);
    wait_flags<64>(rs, WS_FQ, tag, lane, dead);
    STAMP(9);
    if (w == 0) wait_flags<56>(rs, WS_FM, tag, lane, dead);
    STAMP(10);
    f4 xres = {bflo(x0.x), bfhi(x0.x), bflo(x0.y), bfhi(x0.y)};
    const f4 bias2 = {bflo(bb2.x), bfhi(bb2.x), bflo(bb2.y), bfhi(bb2.y)};
    v4u sel;
    {
      const int n = lane & 15;
      sel.x = n == 0 ? 0x00003F80u : n == 1 ? 0x3F800000u : 0u;
      sel.y = n == 2 ? 0x00003F80u : n == 3 ? 0x3F800000u : 0u;
      sel.z = n == 4 ? 0x00003F80u : n == 5 ? 0x3F800000u : 0u;
      sel.w = n == 6 ? 0x00003F80u : n == 7 ? 0x3F800000u : 0u;
    }
    const f4 z = {0.f, 0.f, 0.f, 0.f};
#pragma unroll 1
    for (int l = 0; l < 2; ++l) {
      if (l) sleep64<DLY_Q>(); else sleep64<D_Q0>();
      v4u qv[1];
      gather128<1>(rs, WS_Q + l * Q_BYTES + b * 8192 + g * 2048 + w * 256 + (lane & 15) * 16, 0, qv, dead);
      STAMP(3 + l * 8);
      if (lane < 16) *(LAS v4u*)(L + L_QIMG + w * 288 + lane * 16) = qv[0];
      bar();
      f4 s0 = z, s1 = z;
#pragma unroll
      for (int s = 0; s < 4; ++s) {
        const v4u qf = *(LAS v4u*)(L + L_QIMG + b8 * 288 + s * 64 + gq * 16);
        s0 = mfma(Kf[s], qf, s0); s1 = mfma(Kf[4 + s], qf, s1);
      }
      constexpr float SC = 0.08838834764831845f * 1.4426950408889634f;
      const f4 sc = sel4(half, s1, s0) * SC;
      float mx = fmaxf(fmaxf(sc.x, sc.y), fmaxf(sc.z, sc.w));
      mx = fmaxf(mx, ror8(mx)); mx = x16max(mx); mx = x32max(mx);
      if (lane < 8) *(LAS float*)(L + L_MX + lane * 32 + w * 4) = mx;
      bar();
      {
        const f4 m0 = *(LAS f4*)(L + L_MX + b8 * 32), m1 = *(LAS f4*)(L + L_MX + b8 * 32 + 16);
        mx = fmaxf(fmaxf(fmaxf(m0.x, m0.y), fmaxf(m0.z, m0.w)), fmaxf(fmaxf(m1.x, m1.y), fmaxf(m1.z, m1.w)));
      }
      const float p0 = __builtin_amdgcn_exp2f(sc.x - mx), p1 = __builtin_amdgcn_exp2f(sc.y - mx), p2 = __builtin_amdgcn_exp2f(sc.z - mx), p3 = __builtin_amdgcn_exp2f(sc.w - mx);
      *(LAS v2u*)(L + L_PIMG + ((w * 4 + gq) * 8 + b8) * 16 + half * 8) = v2u{pk2(p0, p1), pk2(p2, p3)};
      float ls = (p0 + p1) + (p2 + p3);
      ls += ror8(ls); ls = x16sum(ls); ls = x32sum(ls);
      if (lane < 8) *(LAS float*)(L + L_LS + lane * 32 + w * 4) = ls;
      bar();
      f4 o0 = z, o1 = z;
#pragma unroll
      for (int ks = 0; ks < 8; ks += 2) {
        const v4u pa = *(LAS v4u*)(L + L_PIMG + ((ks * 4 + gq) * 8 + b8) * 16), pb = *(LAS v4u*)(L + L_PIMG + (((ks + 1) * 4 + gq) * 8 + b8) * 16);
        o0 = mfma(Vf[ks], pa, o0); o1 = mfma(Vf[ks + 1], pb, o1);
      }
      {
        const f4 l0 = *(LAS f4*)(L + L_LS + b8 * 32), l1 = *(LAS f4*)(L + L_LS + b8 * 32 + 16);
        const float tot = ((l0.x + l0.y) + (l0.z + l0.w)) + ((l1.x + l1.y) + (l1.z + l1.w));
        const f4 o = (o0 + o1) * __builtin_amdgcn_rcpf(tot);
        if (!half) st128(rs, WS_A + l * A_BYTES + arow + (16 * w + 4 * gq) * 4,
                         v4u{nosent(__float_as_uint(o.x)), nosent(__float_as_uint(o.y)), nosent(__float_as_uint(o.z)), nosent(__float_as_uint(o.w))});
        if (w == 0 && lane < 8) st32(rs, WS_A + l * A_BYTES + arow + 512, nosent(__float_as_uint(mx + __builtin_amdgcn_logf(tot))));
      }
      STAMP(4 + l * 8);
      if (w == 0) {
        {
          sleep64<DLY_P>();
          v4u pv[4];
          gather128<4>(rs, WS_P + l * P_BYTES + j * 4096 + lane * 16, 1024, pv, dead);
          STAMP(5 + l * 8);
          f4 acc = z;
#pragma unroll
          for (int i = 0; i < 4; ++i) acc = mfma(sel, pv[i], acc);
          xres.x += acc.x + ror8(acc.x); xres.y += acc.y + ror8(acc.y); xres.z += acc.z + ror8(acc.z); xres.w += acc.w + ror8(acc.w);
          if (lane < 32 && !half) st64(rs, WS_X + (2 * l) * X_BYTES + j * 128 + b8 * 16 + gq * 8, v2u{nosent(pk2(xres.x, xres.y)), nosent(pk2(xres.z, xres.w))});
          STAMP(6 + l * 8);
        }
        {
          sleep64<DLY_M>();
          v4u pv[7];
          gather128<7>(rs, WS_M + l * M_BYTES + j * 7168 + lane * 16, 1024, pv, dead);
          STAMP(7 + l * 8);
          f4 acc = z;
#pragma unroll
          for (int i = 0; i < 7; ++i) acc = mfma(sel, pv[i], acc);
          xres.x += (acc.x + ror8(acc.x)) + bias2.x; xres.y += (acc.y + ror8(acc.y)) + bias2.y;
          xres.z += (acc.z + ror8(acc.z)) + bias2.z; xres.w += (acc.w + ror8(acc.w)) + bias2.w;
          const v2u o = {pk2(xres.x, xres.y), pk2(xres.z, xres.w)};
          if (lane < 32 && !half) {
            if (l == 0) st64(rs, WS_X + X_BYTES + j * 128 + b8 * 16 + gq * 8, v2u{nosent(o.x), nosent(o.y)});
            else *(GAS v2u*)(a.out + b8 * (DM * 2) + (8 * j + 4 * gq) * 2) = o;
          }
          STAMP(8 + l * 8);
        }
      }
      bar();
    }
    if (c == 0 && tid == 0) st64(rs, WS_E, v2u{(u32)tag, (u32)(tag >> 32)});
    if ((c == 0 || c == 5) && tid == 0) { const u64 t1 = __builtin_amdgcn_s_memrealtime(), c1 = __builtin_amdgcn_s_memtime(); printf("cu %d: %d ticks(10ns) %d clks -> %f MHz\n", c, (int)(t1 - dbg_t0), (int)(c1 - dbg_c0), 100.0 * (double)(c1 - dbg_c0) / (double)(t1 - dbg_t0)); }
  } else if (r < 24) {
    const int qi = xcd * 8 + (r - 16), h = qi >> 1, ch = qi & 1;
    const v4u x00 = *(const GAS v4u*)(a.x + b8 * (DM * 2) + (128 * w + 32 * half + 8 * gq) * 2);
    const v4u x01 = *(const GAS v4u*)(a.x + b8 * (DM * 2) + (128 * w + 64 + 32 * half + 8 * gq) * 2);
    const u32 qoff = (u32)b8 * 8192 + (64 * qi + 32 * (w & 1) + 8 * gq) * 2;
    const u32 poff = (u32)((64 * ch + 8 * w + 4 * half + gq) * 32 + h) * 128 + b8 * 16;
#pragma unroll
    for (int l = 0; l < NCLR; ++l) {
      st128(rs, WS_P + l * P_BYTES + poff, sent4);
      if (w < 2 && !half) st128(rs, WS_Q + l * Q_BYTES + qoff, sent4);
    }
    slab64_issue(a.wq + (size_t)(128 * w) * 8192 + qi * 128, 8192, lds0 + w * 16384, lane);
    STAMP(17);
    const u64 tag = epoch(a.ws) + 1;
    STAMP(18);
    vmwait<16>();
    STAMP(19);
    bar();
    if (tid == 0) st64(rs, WS_FQ + qi * 8, v2u{(u32)tag, (u32)(tag >> 32)});
    STAMP(1);
    v4u Wq[16], Wo[16];
    slab64_read(L + w * 16384, lane, Wq);
    STAMP(2);
    lin_colsum_partial(L, Wq, lane, w);
    bar();
    f4 cs = {0.f, 0.f, 0.f, 0.f};
    if (w < 2) cs = lin_sum(L, lane, w);
    bar();
    v4u X0 = x00, X1 = x01;
#pragma unroll 1
    for (int l = 0; l < 2; ++l) {
      if (l) {
        sleep64<DLY_X2>();
        v4u xv[2];
        gather128<2>(rs, WS_X + X_BYTES + (16 * w + 4 * half + gq) * 128 + b8 * 16, 1024, xv, dead);
        X0 = xv[0]; X1 = xv[1];
      }
      STAMP(3 + l * 8);
      lin_partial(L, Wq, X0, X1, lane, w);
      bar();
      if (w < 2) {
        const f4 acc = lin_sum(L, lane, w);
        float mean, var;
        lin_stats(L, lane, mean, var);
        const float rstd = __builtin_amdgcn_rsqf(var + 1e-5f);
        const f4 qv = (acc - mean * cs) * rstd;
        const u32 d0 = nosent(pk2(qv.x, qv.y)), d1 = nosent(pk2(qv.z, qv.w));
        const u32 e0 = ror8u(d0), e1 = ror8u(d1);
        if (!half) st128(rs, WS_Q + l * Q_BYTES + qoff, v4u{d0, d1, e0, e1});
      }
      STAMP(4 + l * 8);
      if (l == 0) {
        bar();
        slab64_issue(a.wo + (size_t)(128 * h) * 2048 + (512 * ch + 64 * w) * 2, 2048, lds0 + w * 16384, lane);
        slab64_read(L + w * 16384, lane, Wo);
        wait_flags<128>(rs, WS_FT, tag, lane, dead);
      }
      STAMP(5 + l * 8);
      sleep64<DLY_A>();
      {
        const u32 row = WS_A + l * A_BYTES + (u32)((h * 8 + w) * 4) * A_ROW;
        v2u ov[4]; u32 le;
        for (u32 spins = 0;; ++spins) {
          asm volatile("" ::: "memory");
#pragma unroll
          for (int s = 0; s < 4; ++s) ov[s] = ld64(rs, row + s * A_ROW + lane * 8);
          le = ld32(rs, row + (lane & 3) * A_ROW + 512);
          u32 m = le;
#pragma unroll
          for (int s = 0; s < 4; ++s) m = umax(m, umax(ov[s].x, ov[s].y));
          if (__all(m != SENT) || dead) break;
          if (spins > SPIN_CAP) dead = true;
        }
        STAMP(6 + l * 8);
        const float lf = __uint_as_float(le);
        const float e0 = quadb<0>(lf), e1 = quadb<1>(lf), e2 = quadb<2>(lf), e3 = quadb<3>(lf);
        const float mx = fmaxf(fmaxf(e0, e1), fmaxf(e2, e3));
        const float w0 = __builtin_amdgcn_exp2f(e0 - mx), w1 = __builtin_amdgcn_exp2f(e1 - mx), w2 = __builtin_amdgcn_exp2f(e2 - mx), w3 = __builtin_amdgcn_exp2f(e3 - mx);
        const float inv = __builtin_amdgcn_rcpf((w0 + w1) + (w2 + w3));
        const float oa = (w0 * __uint_as_float(ov[0].x) + w1 * __uint_as_float(ov[1].x)) + (w2 * __uint_as_float(ov[2].x) + w3 * __uint_as_float(ov[3].x));
        const float ob = (w0 * __uint_as_float(ov[0].y) + w1 * __uint_as_float(ov[1].y)) + (w2 * __uint_as_float(ov[2].y) + w3 * __uint_as_float(ov[3].y));
        *(LAS u32*)(L + L_IMG + w * 288 + lane * 4) = pk2(oa * inv, ob * inv);
      }
      bar();
      {
        const f4 z = {0.f, 0.f, 0.f, 0.f};
        f4 acc[4] = {z, z, z, z};
#pragma unroll
        for (int s = 0; s < 4; ++s) {
          const v4u of = *(LAS v4u*)(L + L_IMG + b8 * 288 + s * 64 + gq * 16);
#pragma unroll
          for (int t = 0; t < 4; ++t) acc[t] = mfma(Wo[t * 4 + s], of, acc[t]);
        }
        const f4 lo = sel4(half, acc[2], acc[0]), hi = sel4(half, acc[3], acc[1]);
        st128(rs, WS_P + l * P_BYTES + poff, v4u{nosent(pk2(lo.x, lo.y)), nosent(pk2(lo.z, lo.w)), nosent(pk2(hi.x, hi.y)), nosent(pk2(hi.z, hi.w))});
      }
      STAMP(7 + l * 8);
    }
  } else {
    const int mi = xcd * 7 + (r - 24);
    const v2u bb1 = *(const GAS v2u*)(a.b1 + (64 * mi + 32 * (w & 1) + 8 * gq + 4 * half) * 2);
    const u32 moff = (u32)((16 * w + 4 * half + gq) * 56 + mi) * 128 + b8 * 16;
#pragma unroll
    for (int l = 0; l < NCLR; ++l) {
      st128(rs, WS_M + l * M_BYTES + moff, sent4);
      st128(rs, WS_M + l * M_BYTES + moff + 8 * 56 * 128, sent4);
    }
    slab64_issue(a.w1 + (size_t)(128 * w) * (FF * 2) + mi * 128, FF * 2, lds0 + w * 16384, lane);
    STAMP(17);
    const u64 tag = epoch(a.ws) + 1;
    STAMP(18);
    vmwait<16>();
    STAMP(19);
    bar();
    if (tid == 0) st64(rs, WS_FM + mi * 8, v2u{(u32)tag, (u32)(tag >> 32)});
    STAMP(1);
    v4u W1[16], W2[16];
    slab64_read(L + w * 16384, lane, W1);
    asm volatile("s_waitcnt lgkmcnt(0)" ::: "memory");
    STAMP(2);
    {
      const u32 rr = lane >> 4, cs = lane & 15;
      const u32 voff = rr * 2048 + ((cs ^ (rr << 2)) * 16);
#pragma unroll
      for (int i = 0; i < 16; ++i) dma16(voff, a.w2 + (size_t)(64 * mi + 4 * i) * 2048 + w * 256, lds0 + w * 16384 + i * 1024);
    }
    lin_colsum_partial(L, W1, lane, w);
    bar();
    f4 cs = {0.f, 0.f, 0.f, 0.f};
    if (w < 2) cs = lin_sum(L, lane, w);
    {
      const int q = (lane >> 2) & 3, p = lane & 3;
#pragma unroll
      for (int s = 0; s < 2; ++s) {
        if (s == 0) vmwait<8>(); else vmwait<0>();
#pragma unroll
        for (int u = 0; u < 4; ++u)
#pragma unroll
          for (int hf = 0; hf < 2; ++hf) {
            lptr ad = L + w * 16384 + (32 * s + 8 * gq + q) * 256 + (4 * (u ^ q) + p) * 16 + 8 * hf;
            const v2u lo = trread(ad), hi = trread(ad + 1024);
            W2[(u * 2 + hf) * 2 + s] = v4u{lo.x, lo.y, hi.x, hi.y};
          }
      }
    }
    STAMP(3);
    wait_flags<128>(rs, WS_FT, tag, lane, dead);
    const f4 bias1 = {bflo(bb1.x), bfhi(bb1.x), bflo(bb1.y), bfhi(bb1.y)};
    bar();
#pragma unroll 1
    for (int l = 0; l < 2; ++l) {
      sleep64<DLY_X>();
      v4u xv[2];
      gather128<2>(rs, WS_X + (2 * l) * X_BYTES + (16 * w + 4 * half + gq) * 128 + b8 * 16, 1024, xv, dead);
      STAMP(4 + l * 8);
      lin_partial(L, W1, xv[0], xv[1], lane, w);
      bar();
      if (w < 2) {
        const f4 acc = lin_sum(L, lane, w);
        float mean, var;
        lin_stats(L, lane, mean, var);
        const float r1 = __builtin_amdgcn_rsqf(var + 1e-5f);
        const float r2 = __builtin_amdgcn_rsqf(var * r1 * r1 + 1e-5f);
        const f4 zz = (acc - mean * cs) * (r1 * r2) + bias1;
        float hv[4];
#pragma unroll
        for (int i = 0; i < 4; ++i) {
          const float t = zz[i];
          const float uu = t * (1.0f + 0.044715f * t * t) * (-2.0f * 0.7978845608028654f * 1.4426950408889634f);
          hv[i] = t * __builtin_amdgcn_rcpf(1.0f + __builtin_amdgcn_exp2f(uu));
        }
        *(LAS v2u*)(L + L_IMG + b8 * 160 + (32 * w + 8 * gq + 4 * half) * 2) = v2u{pk2(hv[0], hv[1]), pk2(hv[2], hv[3])};
      }
      bar();
      STAMP(5 + l * 8);
      {
        const f4 z = {0.f, 0.f, 0.f, 0.f};
        const v4u h0 = *(LAS v4u*)(L + L_IMG + b8 * 160 + gq * 16), h1 = *(LAS v4u*)(L + L_IMG + b8 * 160 + 64 + gq * 16);
#pragma unroll
        for (int uu = 0; uu < 2; ++uu) {
          f4 acc[4];
#pragma unroll
          for (int t = 0; t < 4; ++t) { acc[t] = mfma(W2[(uu * 4 + t) * 2], h0, z); acc[t] = mfma(W2[(uu * 4 + t) * 2 + 1], h1, acc[t]); }
          const f4 lo = sel4(half, acc[2], acc[0]), hi = sel4(half, acc[3], acc[1]);
          st128(rs, WS_M + l * M_BYTES + moff + uu * (8 * 56 * 128),
                v4u{nosent(pk2(lo.x, lo.y)), nosent(pk2(lo.z, lo.w)), nosent(pk2(hi.x, hi.y)), nosent(pk2(hi.z, hi.w))});
        }
      }
      STAMP(6 + l * 8);
    }
  }
}
}

extern "C" void kernel_launch(void* const* d_in, const int* in_sizes, int n_in, void* d_out, int out_size, void* d_ws, size_t ws_size,
                              hipStream_t stream) {
  Args a;
  a.x = (const unsigned char*)d_in[0];
  a.kc = (const unsigned char*)d_in[1];
  a.vc = (const unsigned char*)d_in[2];
  a.wq = (const unsigned char*)d_in[3];
  a.wo = (const unsigned char*)d_in[4];
  a.w1 = (const unsigned char*)d_in[6];
  a.b1 = (const unsigned char*)d_in[7];
  a.w2 = (const unsigned char*)d_in[8];
  a.b2 = (const unsigned char*)d_in[9];
  a.out = (unsigned char*)d_out;
  a.ws = (unsigned char*)d_ws;
  (void)hipFuncSetAttribute((const void*)fused_decode, hipFuncAttributeMaxDynamicSharedMemorySize, LDS_BYTES);
  hipLaunchKernelGGL(fused_decode, dim3(256), dim3(512), LDS_BYTES, stream, a);
}
```

```cpp
#include <hip/hip_runtime.h>
#include <stdint.h>

namespace {
typedef unsigned u32;
typedef unsigned long long u64;
typedef u32 v4u __attribute__((ext_vector_type(4)));
typedef u32 v2u __attribute__((ext_vector_type(2)));
typedef float f4 __attribute__((ext_vector_type(4)));
typedef __bf16 bf8 __attribute__((ext_vector_type(8)));
typedef __bf16 bf2 __attribute__((ext_vector_type(2)));
typedef short s4 __attribute__((ext_vector_type(4)));
#define LAS __attribute__((address_space(3)))
#define GAS __attribute__((address_space(1)))
typedef LAS unsigned char* lptr;
typedef __amdgpu_buffer_rsrc_t rsrc_t;

constexpr int DM = 1024, SK = 4096, FF = 3584;
constexpr u32 SENT = 0xFFFFFFFFu;
constexpr int SC1 = 16;
constexpr u32 SPIN_CAP = 1u << 17;
constexpr int PACER = 19;

constexpr u32 WS_E = 0;
constexpr u32 WS_F = 1024;
constexpr u32 X_BYTES = 16384, Q_BYTES = 65536, A_ROW = 512, A_BYTES = 32 * 8 * 4 * A_ROW, P_BYTES = 128 * 32 * 128, M_BYTES = 128 * 56 * 128;
constexpr u32 WS_S0 = 8192;
constexpr u32 WS_X = WS_S0;
constexpr u32 WS_T0 = WS_X + 3 * X_BYTES;
constexpr u32 WS_Q0 = WS_X + 3 * X_BYTES + 16384;
constexpr u32 WS_A0 = WS_Q0 + Q_BYTES;
constexpr u32 WS_S1 = WS_A0 + A_BYTES;
constexpr u32 WS_P0 = WS_S1;
constexpr u32 WS_M0 = WS_P0 + P_BYTES;
constexpr u32 WS_S2 = WS_M0 + M_BYTES;
constexpr u32 WS_Q1 = WS_S2, WS_A1 = WS_Q1 + Q_BYTES;
constexpr u32 WS_S3 = WS_A1 + A_BYTES;
constexpr u32 WS_P1 = WS_S3, WS_M1 = WS_P1 + P_BYTES;
constexpr u32 WS_END = WS_M1 + M_BYTES;
constexpr u32 WS_ST = WS_END;
static_assert((WS_S1 - WS_S0) % 65536 == 0 && (WS_S2 - WS_S1) % 65536 == 0 && (WS_S3 - WS_S2) % 65536 == 0 && (WS_END - WS_S3) % 65536 == 0, "64 waves x 1 KB pieces");

constexpr u32 L_WK = 131072;
constexpr u32 L_RED = L_WK, L_STAT = L_WK + 16384, L_IMG = L_WK + 17408;
constexpr u32 L_QIMG = L_WK, L_MX = L_WK + 2560, L_LS = L_WK + 2816, L_PIMG = L_WK + 3072, L_RA = L_WK + 7168, L_RB = L_WK + 9216;
constexpr u32 L_STAMP = L_WK + 20480;
constexpr u32 LDS_BYTES = L_WK + 20480 + 2048;

enum { P_T0, P_M0, P_C2, P_A0, P_P0, P_M0W, P_X2, P_Q1, P_A1, P_P1, P_X1, P_M1W, P_N };
#define PACE 0, 400, 400, 0, 0, 143, 213, 358, 504, 618, 688, 865
constexpr u32 kPace[P_N] = {PACE};
#define PACEV(k) (kPace[k])

#define STAMP(i) do { } while (0)

__device__ __forceinline__ void bar() { asm volatile("s_waitcnt lgkmcnt(0)\n\ts_barrier" ::: "memory"); }
__device__ __forceinline__ void lgkm0() { asm volatile("s_waitcnt lgkmcnt(0)" ::: "memory"); }
template <int N> __device__ __forceinline__ void vmwait() { asm volatile("s_waitcnt vmcnt(%0)" ::"i"(N) : "memory"); }
__device__ __forceinline__ u32 now() { return (u32)__builtin_amdgcn_s_memrealtime(); }
__device__ __forceinline__ void until(u32 ref, u32 d) {
  if (d) { const u32 t0 = now(); for (u32 t = t0; (int)(t - ref) < (int)d && t - t0 < 2 * d; t = now()) { } }
}

__device__ __forceinline__ void dma16(u32 voff, const void* sbase, u32 dst) {
  u32 keep;
  asm volatile("s_mov_b32 %0, m0\n\ts_mov_b32 m0, %3\n\ts_nop 2\n\tglobal_load_lds_dwordx4 %1, %2 nt\n\ts_mov_b32 m0, %0"
               : "=&s"(keep) : "v"(voff), "s"(sbase), "s"(dst) : "memory");
}
__device__ __forceinline__ f4 mfma(v4u a, v4u b, f4 c) {
  return __builtin_amdgcn_mfma_f32_16x16x32_bf16(__builtin_bit_cast(bf8, a), __builtin_bit_cast(bf8, b), c, 0, 0, 0);
}
__device__ __forceinline__ v2u trread(lptr p) { return __builtin_bit_cast(v2u, __builtin_amdgcn_ds_read_tr16_b64_v4i16((LAS s4*)p)); }
__device__ __forceinline__ u32 pk2(float a, float b) { bf2 v = {(__bf16)a, (__bf16)b}; return __builtin_bit_cast(u32, v); }
__device__ __forceinline__ u32 nosent(u32 v) { return v < 0xFFFFFFFEu ? v : 0xFFFFFFFEu; }
__device__ __forceinline__ float bflo(u32 v) { return __uint_as_float(v << 16); }
__device__ __forceinline__ float bfhi(u32 v) { return __uint_as_float(v & 0xFFFF0000u); }
__device__ __forceinline__ u32 ror8u(u32 v) { return (u32)__builtin_amdgcn_mov_dpp((int)v, 0x128, 0xf, 0xf, true); }
__device__ __forceinline__ float ror8(float v) { return __uint_as_float(ror8u(__float_as_uint(v))); }
__device__ __forceinline__ float vmax(float a, float b) { float r; asm("v_max_f32 %0, %1, %2" : "=v"(r) : "v"(a), "v"(b)); return r; }
__device__ __forceinline__ float vmax3(float a, float b, float c) { float r; asm("v_max3_f32 %0, %1, %2, %3" : "=v"(r) : "v"(a), "v"(b), "v"(c)); return r; }
__device__ __forceinline__ void sched() { __builtin_amdgcn_sched_barrier(0); }
__device__ __forceinline__ float x16max(float x) { auto s = __builtin_amdgcn_permlane16_swap(__float_as_uint(x), __float_as_uint(x), false, false); return vmax(__uint_as_float(s[0]), __uint_as_float(s[1])); }
__device__ __forceinline__ float x32max(float x) { auto s = __builtin_amdgcn_permlane32_swap(__float_as_uint(x), __float_as_uint(x), false, false); return vmax(__uint_as_float(s[0]), __uint_as_float(s[1])); }
__device__ __forceinline__ float x16sum(float x) { auto s = __builtin_amdgcn_permlane16_swap(__float_as_uint(x), __float_as_uint(x), false, false); return __uint_as_float(s[0]) + __uint_as_float(s[1]); }
__device__ __forceinline__ float x32sum(float x) { auto s = __builtin_amdgcn_permlane32_swap(__float_as_uint(x), __float_as_uint(x), false, false); return __uint_as_float(s[0]) + __uint_as_float(s[1]); }
__device__ __forceinline__ f4 sel4(bool c, f4 t, f4 f) { f4 r; r.x = c ? t.x : f.x; r.y = c ? t.y : f.y; r.z = c ? t.z : f.z; r.w = c ? t.w : f.w; return r; }
__device__ __forceinline__ u32 umax(u32 a, u32 b) { return a > b ? a : b; }
__device__ __forceinline__ u32 max4(v4u v) { return umax(umax(v.x, v.y), umax(v.z, v.w)); }
__device__ __forceinline__ v4u ld128(rsrc_t rs, u32 off) { return __builtin_amdgcn_raw_buffer_load_b128(rs, off, 0, SC1); }
__device__ __forceinline__ v2u ld64(rsrc_t rs, u32 off) { return __builtin_amdgcn_raw_buffer_load_b64(rs, off, 0, SC1); }
__device__ __forceinline__ void st128(rsrc_t rs, u32 off, v4u v) { __builtin_amdgcn_raw_buffer_store_b128(v, rs, off, 0, SC1); }
__device__ __forceinline__ void st64(rsrc_t rs, u32 off, v2u v) { __builtin_amdgcn_raw_buffer_store_b64(v, rs, off, 0, SC1); }

__device__ __forceinline__ u64 epoch(const unsigned char* ws) {
  u64 e;
  asm volatile("s_load_dwordx2 %0, %1, 0x0\n\ts_waitcnt lgkmcnt(0)" : "=&s"(e) : "s"(ws) : "memory");
  return e;
}
__device__ __forceinline__ void wait_stage(rsrc_t rs, int stage, u64 tag, int lane, bool& dead) {
  const u32 lo = (u32)tag, hi = (u32)(tag >> 32);
  for (u32 spins = 0; !dead; ++spins) {
    asm volatile("" ::: "memory");
    const v2u v = ld64(rs, WS_F + stage * 64 + (lane & 7) * 8);
    if (__all(v.x == lo && v.y == hi)) break;
    if (spins > SPIN_CAP) dead = true;
  }
}
__device__ __forceinline__ u32 wait_t0(rsrc_t rs, bool& dead) {
  v2u v = {0u, 0u};
  for (u32 spins = 0; !dead; ++spins) {
    asm volatile("" ::: "memory");
    v = ld64(rs, WS_T0);
    if (__all(v.x != SENT && v.y != SENT)) break;
    if (spins > SPIN_CAP) dead = true;
  }
  return __builtin_amdgcn_readfirstlane(v.x);
}
template <int N> __device__ __forceinline__ void gather128(rsrc_t rs, u32 off, u32 stride, v4u (&v)[N], bool& dead) {
  for (u32 spins = 0;; ++spins) {
    asm volatile("" ::: "memory");
#pragma unroll
    for (int i = 0; i < N; ++i) v[i] = ld128(rs, off + i * stride);
    u32 m = max4(v[0]);
#pragma unroll
    for (int i = 1; i < N; ++i) m = umax(m, max4(v[i]));
    if (__all(m != SENT) || dead) break;
    if (spins > SPIN_CAP) dead = true;
  }
}

template <int G> __device__ __forceinline__ void slab64_issue4(const unsigned char* gbase, u32 rowb, u32 ldsw, int lane) {
  const u32 voff = (u32)(lane >> 3) * rowb + (u32)(((lane & 7) ^ (((lane >> 4) & 1) << 2)) * 16);
#pragma unroll
  for (int i = 4 * G; i < 4 * G + 4; ++i) dma16(voff, gbase + (size_t)i * 8 * rowb, ldsw + i * 1024);
}
template <int S> __device__ __forceinline__ void slab64_read1(lptr Lw, int lane, v4u (&A)[16]) {
  const int gq = lane >> 4, q = (lane >> 2) & 3, p = lane & 3, t = q >> 1;
  lptr a0 = Lw + (8 * gq + q) * 128 + (4 * t + p) * 16, a1 = Lw + (8 * gq + q) * 128 + (4 * (1 - t) + p) * 16;
#pragma unroll
  for (int u = 0; u < 2; ++u)
#pragma unroll
    for (int hf = 0; hf < 2; ++hf) {
      lptr a = (u ? a1 : a0) + S * 4096 + 8 * hf;
      const v2u lo = trread(a), hi = trread(a + 512);
      A[(u * 2 + hf) * 4 + S] = v4u{lo.x, lo.y, hi.x, hi.y};
    }
}
template <int G> __device__ __forceinline__ void rows1k_issue4(const unsigned char* gbase, u32 ldsw, int lane) {
#pragma unroll
  for (int i = 4 * G; i < 4 * G + 4; ++i) dma16((u32)(lane ^ (4 * (i & 3))) * 16, gbase + (size_t)i * 2048, ldsw + i * 1024);
}
__device__ __forceinline__ void rows1k_read(lptr L, int lane, int w, v4u (&A)[16]) {
  const int gq = lane >> 4, q = (lane >> 2) & 3, p = lane & 3;
#pragma unroll
  for (int u = 0; u < 2; ++u) {
    lptr ad = L + (8 * gq + q) * 1024 + (((8 * w + 4 * u) ^ (4 * q)) + p) * 16;
#pragma unroll
    for (int hf = 0; hf < 2; ++hf)
#pragma unroll
      for (int s = 0; s < 4; ++s) {
        const v2u lo = trread(ad + s * 32768 + 8 * hf), hi = trread(ad + s * 32768 + 4096 + 8 * hf);
        A[(u * 2 + hf) * 4 + s] = v4u{lo.x, lo.y, hi.x, hi.y};
      }
  }
}
template <int G> __device__ __forceinline__ void rows2k_issue4(const unsigned char* gbase, u32 ldsw, int lane) {
#pragma unroll
  for (int i = 4 * G; i < 4 * G + 4; ++i) dma16((u32)(lane ^ (4 * ((i >> 1) & 3))) * 16, gbase + (size_t)i * 1024, ldsw + i * 1024);
}
__device__ __forceinline__ void rows2k_read(lptr L, int lane, int w, v4u (&A)[16]) {
  const int gq = lane >> 4, q = (lane >> 2) & 3, p = lane & 3;
#pragma unroll
  for (int u = 0; u < 4; ++u) {
    lptr ad = L + (8 * gq + q) * 2048 + (16 * w + 4 * (u ^ q) + p) * 16;
#pragma unroll
    for (int hf = 0; hf < 2; ++hf)
#pragma unroll
      for (int s = 0; s < 2; ++s) {
        const v2u lo = trread(ad + s * 65536 + 8 * hf), hi = trread(ad + s * 65536 + 8192 + 8 * hf);
        A[(u * 2 + hf) * 2 + s] = v4u{lo.x, lo.y, hi.x, hi.y};
      }
  }
}

__device__ __forceinline__ void lin_partial(lptr L, const v4u (&A)[16], v4u X0, v4u X1, int lane, int w) {
  const f4 z = {0.f, 0.f, 0.f, 0.f};
  const v4u ones = {0x3F803F80u, 0x3F803F80u, 0x3F803F80u, 0x3F803F80u};
  f4 G = mfma(X0, X0, z), S = mfma(ones, X0, z);
  G = mfma(X1, X1, G); S = mfma(ones, X1, S);
  const v4u R0 = {ror8u(X0.x), ror8u(X0.y), ror8u(X0.z), ror8u(X0.w)}, R1 = {ror8u(X1.x), ror8u(X1.y), ror8u(X1.z), ror8u(X1.w)};
  __builtin_amdgcn_sched_barrier(0);
  f4 acc[4];
#pragma unroll
  for (int u = 0; u < 2; ++u) {
    f4 a = mfma(A[(u * 2) * 4 + 0], X0, z), c = mfma(A[(u * 2 + 1) * 4 + 1], X0, z);
    a = mfma(A[(u * 2) * 4 + 2], X1, a); c = mfma(A[(u * 2 + 1) * 4 + 3], X1, c);
    a = mfma(A[(u * 2) * 4 + 1], R0, a); c = mfma(A[(u * 2 + 1) * 4 + 0], R0, c);
    a = mfma(A[(u * 2) * 4 + 3], R1, a); c = mfma(A[(u * 2 + 1) * 4 + 2], R1, c);
    acc[u * 2] = a; acc[u * 2 + 1] = c;
    if (u == 0) {
      const int n = lane & 15;
      const float g01 = (lane & 1) ? G.y : G.x, g23 = (lane & 1) ? G.w : G.z, g = (lane & 2) ? g23 : g01;
      if ((lane >> 4) == (n >> 2)) *(LAS v2u*)(L + L_STAT + n * 64 + w * 8) = v2u{__float_as_uint(S.x), __float_as_uint(g)};
    }
  }
  const bool half = (lane >> 3) & 1;
#pragma unroll
  for (int u = 0; u < 2; ++u) *(LAS f4*)(L + L_RED + (w * 2 + u) * 1024 + lane * 16) = sel4(half, acc[u * 2 + 1], acc[u * 2]);
}
__device__ __forceinline__ void lin_colsum_partial(lptr L, const v4u (&A)[16], int lane, int w) {
  const f4 z = {0.f, 0.f, 0.f, 0.f};
  const v4u ones = {0x3F803F80u, 0x3F803F80u, 0x3F803F80u, 0x3F803F80u};
  const bool half = (lane >> 3) & 1;
#pragma unroll
  for (int u = 0; u < 2; ++u) {
    f4 a = z, c = z;
#pragma unroll
    for (int s = 0; s < 4; ++s) { a = mfma(A[(u * 2) * 4 + s], ones, a); c = mfma(A[(u * 2 + 1) * 4 + s], ones, c); }
    *(LAS f4*)(L + L_RED + (w * 2 + u) * 1024 + lane * 16) = sel4(half, c, a);
  }
}
__device__ __forceinline__ f4 lin_sum(lptr L, int lane, int u) {
  f4 v[8];
#pragma unroll
  for (int i = 0; i < 8; ++i) v[i] = *(LAS f4*)(L + L_RED + (i * 2 + u) * 1024 + lane * 16);
  return ((v[0] + v[1]) + (v[2] + v[3])) + ((v[4] + v[5]) + (v[6] + v[7]));
}
__device__ __forceinline__ f4 lin_final(lptr L, int lane, int u, float& mean, float& var) {
  f4 t[4], v[8];
#pragma unroll
  for (int i = 0; i < 4; ++i) t[i] = *(LAS f4*)(L + L_STAT + (lane & 15) * 64 + i * 16);
#pragma unroll
  for (int i = 0; i < 8; ++i) v[i] = *(LAS f4*)(L + L_RED + (i * 2 + u) * 1024 + lane * 16);
  sched();
  float s1 = ((t[0].x + t[0].z) + (t[1].x + t[1].z)) + ((t[2].x + t[2].z) + (t[3].x + t[3].z));
  float s2 = ((t[0].y + t[0].w) + (t[1].y + t[1].w)) + ((t[2].y + t[2].w) + (t[3].y + t[3].w));
  s1 += ror8(s1); s2 += ror8(s2);
  mean = s1 * (1.0f / DM);
  var = fmaxf(s2 * (1.0f / DM) - mean * mean, 0.f);
  return ((v[0] + v[1]) + (v[2] + v[3])) + ((v[4] + v[5]) + (v[6] + v[7]));
}

struct Args {
  const unsigned char *x, *kc, *vc, *wq, *wo, *w1, *b1, *w2, *b2;
  unsigned char *out, *ws;
};

__global__ void __launch_bounds__(512, 2) fused_decode(Args a) {
  extern __shared__ __attribute__((aligned(16))) unsigned char smem[];
  lptr L = (lptr)smem;
  const u32 lds0 = (u32)(uintptr_t)L;
  const int tid = threadIdx.x, lane = tid & 63, w = __builtin_amdgcn_readfirstlane(tid >> 6);
  const int c = blockIdx.x, xcd = c & 7, r = c >> 3;
  STAMP(0);
  const rsrc_t rs = __builtin_amdgcn_make_buffer_rsrc((void*)a.ws, 0, WS_END, 0x00020000);
  const int gq = lane >> 4, half = (lane >> 3) & 1, b8 = lane & 7;
  const f4 z = {0.f, 0.f, 0.f, 0.f};
  bool dead = false;
  const u32 t_start = now();

  if (r < 16) {
    const int b = xcd, g = r >> 2, sp = r & 3, j = xcd * 16 + r;
    const u32 colb = (u32)(8 * j + 4 * (gq & 1)) * 2;
    const v2u x0 = *(const GAS v2u*)(a.x + b8 * (DM * 2) + colb), bb2 = *(const GAS v2u*)(a.b2 + colb);
    until(t_start, PACEV(P_T0));
    const size_t kvrow = (size_t)((b * 4 + g) * SK + (SK - 1024) + 256 * sp + 32 * w) * 256;
    {
      const u32 rr = lane >> 4, cs = lane & 15;
      const u32 vk = rr * 256 + ((cs ^ rr) * 16), vv = rr * 256 + ((cs ^ (rr << 1)) * 16);
#pragma unroll
      for (int i = 0; i < 8; ++i) dma16(vk ^ ((i & 3) << 6), a.kc + kvrow + i * 1024, lds0 + w * 8192 + i * 1024);
#pragma unroll
      for (int i = 0; i < 8; ++i) dma16(vv ^ ((i & 1) << 7), a.vc + kvrow + i * 1024, lds0 + 65536 + w * 8192 + i * 1024);
    }
    const u64 tag = epoch(a.ws) + 1;
    STAMP(1);
    v4u Kf[8], Vf[8];
    vmwait<8>();
#pragma unroll
    for (int tk = 0; tk < 2; ++tk)
#pragma unroll
      for (int s = 0; s < 4; ++s)
        Kf[tk * 4 + s] = *(LAS v4u*)(L + w * 8192 + tk * 4096 + (lane & 15) * 256 + (((gq ^ (lane & 15)) ^ (4 * s)) * 16));
    wait_stage(rs, 0, tag, lane, dead);
    vmwait<0>();
    bar();
    {
      const int q = (lane >> 2) & 3, p = lane & 3;
      lptr vb = L + 65536 + (4 * gq + q) * 256 + (((2 * w + (p >> 1)) ^ ((4 * (gq & 1) + q) << 1)) * 16) + 8 * (p & 1);
#pragma unroll
      for (int ks = 0; ks < 8; ++ks) {
        const v2u lo = trread(vb + ks * 8192), hi = trread(vb + ks * 8192 + 4096);
        Vf[ks] = v4u{lo.x, lo.y, hi.x, hi.y};
      }
    }
    STAMP(2);
    f4 xres = {bflo(x0.x), bfhi(x0.x), bflo(x0.y), bfhi(x0.y)};
    const f4 bias2 = {bflo(bb2.x), bfhi(bb2.x), bflo(bb2.y), bfhi(bb2.y)};
    v4u sel;
    {
      const int n = lane & 15;
      sel.x = n == 0 ? 0x00003F80u : n == 1 ? 0x3F800000u : 0u;
      sel.y = n == 2 ? 0x00003F80u : n == 3 ? 0x3F800000u : 0u;
      sel.z = n == 4 ? 0x00003F80u : n == 5 ? 0x3F800000u : 0u;
      sel.w = n == 6 ? 0x00003F80u : n == 7 ? 0x3F800000u : 0u;
    }
    const u32 arow = (u32)(((g * 8 + b8) * 8 + b) * 4 + sp) * A_ROW + (16 * w + 4 * gq) * 4;
    u32 tref = 0, T0 = 0;
#pragma unroll 1
    for (int l = 0; l < 2; ++l) {
      if (l) { wait_stage(rs, 2, tag, lane, dead); until(T0, PACEV(P_Q1)); }
      v4u qv[1];
      gather128<1>(rs, (l ? WS_Q1 : WS_Q0) + b * 8192 + g * 2048 + w * 256 + (lane & 15) * 16, 0, qv, dead);
      STAMP(3 + l * 8);
      if (lane < 16) *(LAS v4u*)(L + L_QIMG + w * 288 + lane * 16) = qv[0];
      bar();
      tref = now();
      f4 s0 = z, s1 = z;
      {
        v4u qf[4];
#pragma unroll
        for (int s = 0; s < 4; ++s) qf[s] = *(LAS v4u*)(L + L_QIMG + b8 * 288 + s * 64 + gq * 16);
        sched();
#pragma unroll
        for (int s = 0; s < 4; ++s) { s0 = mfma(Kf[s], qf[s], s0); s1 = mfma(Kf[4 + s], qf[s], s1); }
      }
      constexpr float SC = 0.08838834764831845f * 1.4426950408889634f;
      const f4 sc = sel4(half, s1, s0) * SC;
      float mx = vmax3(vmax(sc.x, sc.y), sc.z, sc.w);
      mx = vmax(mx, ror8(mx)); mx = x16max(mx); mx = x32max(mx);
      if (lane < 8) *(LAS float*)(L + L_MX + lane * 32 + w * 4) = mx;
      bar();
      {
        const f4 m0 = *(LAS f4*)(L + L_MX + b8 * 32), m1 = *(LAS f4*)(L + L_MX + b8 * 32 + 16);
        mx = vmax3(vmax3(m0.x, m0.y, m0.z), vmax3(m0.w, m1.x, m1.y), vmax(m1.z, m1.w));
      }
      const float p0 = __builtin_amdgcn_exp2f(sc.x - mx), p1 = __builtin_amdgcn_exp2f(sc.y - mx), p2 = __builtin_amdgcn_exp2f(sc.z - mx), p3 = __builtin_amdgcn_exp2f(sc.w - mx);
      *(LAS v2u*)(L + L_PIMG + ((w * 4 + gq) * 8 + b8) * 16 + half * 8) = v2u{pk2(p0, p1), pk2(p2, p3)};
      float ls = (p0 + p1) + (p2 + p3);
      ls += ror8(ls); ls = x16sum(ls); ls = x32sum(ls);
      if (lane < 8) *(LAS float*)(L + L_LS + lane * 32 + w * 4) = ls;
      bar();
      f4 o0 = z, o1 = z;
      {
        v4u pf[8];
#pragma unroll
        for (int ks = 0; ks < 8; ++ks) pf[ks] = *(LAS v4u*)(L + L_PIMG + ((ks * 4 + gq) * 8 + b8) * 16);
        const f4 l0 = *(LAS f4*)(L + L_LS + b8 * 32), l1 = *(LAS f4*)(L + L_LS + b8 * 32 + 16);
        sched();
#pragma unroll
        for (int ks = 0; ks < 8; ks += 2) { o0 = mfma(Vf[ks], pf[ks], o0); o1 = mfma(Vf[ks + 1], pf[ks + 1], o1); }
        const float tot = ((l0.x + l0.y) + (l0.z + l0.w)) + ((l1.x + l1.y) + (l1.z + l1.w));
        const f4 o = (o0 + o1) * __builtin_amdgcn_rcpf(tot);
        const u32 lse = nosent(__float_as_uint(mx + __builtin_amdgcn_logf(tot)));
        if (!half) st128(rs, (l ? WS_A1 : WS_A0) + arow, v4u{nosent(pk2(o.x, o.y)), lse, nosent(pk2(o.z, o.w)), lse});
      }
      STAMP(4 + l * 8);
      if (w == 0) {
        {
          wait_stage(rs, l ? 3 : 1, tag, lane, dead);
          if (l) until(T0, PACEV(P_P1)); else until(tref, PACEV(P_P0));
          v4u pv[4];
          gather128<4>(rs, (l ? WS_P1 : WS_P0) + j * 4096 + lane * 16, 1024, pv, dead);
          STAMP(5 + l * 8);
          f4 acc = mfma(sel, pv[0], z), acc2 = mfma(sel, pv[1], z);
          acc = mfma(sel, pv[2], acc); acc2 = mfma(sel, pv[3], acc2);
          acc += acc2;
          xres.x += acc.x + ror8(acc.x); xres.y += acc.y + ror8(acc.y); xres.z += acc.z + ror8(acc.z); xres.w += acc.w + ror8(acc.w);
          if (lane < 32 && !half) st64(rs, WS_X + (2 * l) * X_BYTES + j * 128 + b8 * 16 + gq * 8, v2u{nosent(pk2(xres.x, xres.y)), nosent(pk2(xres.z, xres.w))});
          STAMP(6 + l * 8);
        }
        {
          if (l == 0) T0 = wait_t0(rs, dead);
          until(T0, l ? PACEV(P_M1W) : PACEV(P_M0W));
          v4u pv[7];
          gather128<7>(rs, (l ? WS_M1 : WS_M0) + j * 7168 + lane * 16, 1024, pv, dead);
          STAMP(7 + l * 8);
          f4 acc = mfma(sel, pv[0], z), acc2 = mfma(sel, pv[1], z);
          acc = mfma(sel, pv[2], acc); acc2 = mfma(sel, pv[3], acc2);
          acc = mfma(sel, pv[4], acc); acc2 = mfma(sel, pv[5], acc2);
          acc = mfma(sel, pv[6], acc);
          acc += acc2;
          xres.x += (acc.x + ror8(acc.x)) + bias2.x; xres.y += (acc.y + ror8(acc.y)) + bias2.y;
          xres.z += (acc.z + ror8(acc.z)) + bias2.z; xres.w += (acc.w + ror8(acc.w)) + bias2.w;
          const v2u o = {pk2(xres.x, xres.y), pk2(xres.z, xres.w)};
          if (lane < 32 && !half) {
            if (l == 0) st64(rs, WS_X + X_BYTES + j * 128 + b8 * 16 + gq * 8, v2u{nosent(o.x), nosent(o.y)});
            else *(GAS v2u*)(a.out + b8 * (DM * 2) + (8 * j + 4 * gq) * 2) = o;
          }
          STAMP(8 + l * 8);
        }
      }
      if (l == 0) { bar(); if (w) T0 = wait_t0(rs, dead); }
    }
    if (c == 0 && tid == 0) st64(rs, WS_E, v2u{(u32)tag, (u32)(tag >> 32)});
  } else if (r < 24) {
    const int qi = xcd * 8 + (r - 16), h = qi >> 1, ch = qi & 1;
    const v4u x00 = *(const GAS v4u*)(a.x + b8 * (DM * 2) + (128 * w + 32 * half + 8 * gq) * 2);
    const v4u x01 = *(const GAS v4u*)(a.x + b8 * (DM * 2) + (128 * w + 64 + 32 * half + 8 * gq) * 2);
    const unsigned char* gq_ = a.wq + (size_t)(128 * w) * 8192 + qi * 128;
    const unsigned char* go_ = a.wo + (size_t)(128 * h + 16 * w) * 2048 + ch * 1024;
    const u32 ldsw = lds0 + w * 16384;
    lptr Lw = L + w * 16384;
    slab64_issue4<0>(gq_, 8192, ldsw, lane); slab64_issue4<1>(gq_, 8192, ldsw, lane);
    slab64_issue4<2>(gq_, 8192, ldsw, lane); slab64_issue4<3>(gq_, 8192, ldsw, lane);
    const u64 tag = epoch(a.ws) + 1;
    STAMP(1);
    v4u Wq[16], Wo[16];
    vmwait<12>(); slab64_read1<0>(Lw, lane, Wq);
    vmwait<8>();  slab64_read1<1>(Lw, lane, Wq);
    vmwait<4>();  slab64_read1<2>(Lw, lane, Wq);
    if (w >= 2) { lgkm0(); rows1k_issue4<0>(go_, ldsw, lane); vmwait<4>(); }
    else { wait_stage(rs, 0, tag, lane, dead); vmwait<0>(); }
    slab64_read1<3>(Lw, lane, Wq);
    STAMP(2);
    lin_colsum_partial(L, Wq, lane, w);
    bar();
    f4 cs = z;
    if (w < 2) cs = lin_sum(L, lane, w);
    bar();
    const u32 qoff = (u32)b8 * 8192 + (64 * qi + 32 * (w & 1) + 8 * gq) * 2;
    const u32 poff = (u32)((64 * ch + 8 * w + 4 * half + gq) * 32 + h) * 128 + b8 * 16;
    v4u X0 = x00, X1 = x01;
    u32 tref = 0, T0 = 0;
#pragma unroll 1
    for (int l = 0; l < 2; ++l) {
      if (l) {
        wait_stage(rs, 2, tag, lane, dead);
        T0 = wait_t0(rs, dead);
        until(T0, PACEV(P_X2));
        v4u xv[2];
        gather128<2>(rs, WS_X + X_BYTES + (16 * w + 4 * half + gq) * 128 + b8 * 16, 1024, xv, dead);
        X0 = xv[0]; X1 = xv[1];
      }
      STAMP(3 + l * 8);
      lin_partial(L, Wq, X0, X1, lane, w);
      bar();
      tref = now();
      if (w < 2) {
        float mean, var;
        const f4 acc = lin_final(L, lane, w, mean, var);
        const float rstd = __builtin_amdgcn_rsqf(var + 1e-5f);
        const f4 qv = (acc - mean * cs) * rstd;
        const u32 d0 = nosent(pk2(qv.x, qv.y)), d1 = nosent(pk2(qv.z, qv.w));
        const u32 e0 = ror8u(d0), e1 = ror8u(d1);
        if (!half) st128(rs, (l ? WS_Q1 : WS_Q0) + qoff, v4u{d0, d1, e0, e1});
      }
      STAMP(4 + l * 8);
      if (l == 0) {
        if (w < 2) rows1k_issue4<0>(go_, ldsw, lane);
        rows1k_issue4<1>(go_, ldsw, lane); rows1k_issue4<2>(go_, ldsw, lane); rows1k_issue4<3>(go_, ldsw, lane);
        vmwait<0>();
        bar();
        rows1k_read(L, lane, w, Wo);
      }
      wait_stage(rs, l ? 3 : 1, tag, lane, dead);
      STAMP(5 + l * 8);
      if (l) until(T0, PACEV(P_A1)); else until(tref, PACEV(P_A0));
      {
        const u32 row = (l ? WS_A1 : WS_A0) + (u32)((h * 8 + w) * 4) * A_ROW + lane * 8;
        v2u ov[4];
        for (u32 spins = 0;; ++spins) {
          asm volatile("" ::: "memory");
#pragma unroll
          for (int s = 0; s < 4; ++s) ov[s] = ld64(rs, row + s * A_ROW);
          u32 m = umax(ov[0].x, ov[0].y);
#pragma unroll
          for (int s = 1; s < 4; ++s) m = umax(m, umax(ov[s].x, ov[s].y));
          if (__all(m != SENT) || dead) break;
          if (spins > SPIN_CAP) dead = true;
        }
        STAMP(6 + l * 8);
        const float e0 = __uint_as_float(ov[0].y), e1 = __uint_as_float(ov[1].y), e2 = __uint_as_float(ov[2].y), e3 = __uint_as_float(ov[3].y);
        const float mx = vmax3(vmax(e0, e1), e2, e3);
        const float w0 = __builtin_amdgcn_exp2f(e0 - mx), w1 = __builtin_amdgcn_exp2f(e1 - mx), w2 = __builtin_amdgcn_exp2f(e2 - mx), w3 = __builtin_amdgcn_exp2f(e3 - mx);
        const float inv = __builtin_amdgcn_rcpf((w0 + w1) + (w2 + w3));
        const float oa = (w0 * bflo(ov[0].x) + w1 * bflo(ov[1].x)) + (w2 * bflo(ov[2].x) + w3 * bflo(ov[3].x));
        const float ob = (w0 * bfhi(ov[0].x) + w1 * bfhi(ov[1].x)) + (w2 * bfhi(ov[2].x) + w3 * bfhi(ov[3].x));
        *(LAS u32*)(L + L_IMG + w * 288 + lane * 4) = pk2(oa * inv, ob * inv);
      }
      bar();
      {
        f4 acc[4] = {z, z, z, z};
        v4u of[4];
#pragma unroll
        for (int s = 0; s < 4; ++s) of[s] = *(LAS v4u*)(L + L_IMG + b8 * 288 + s * 64 + gq * 16);
        sched();
#pragma unroll
        for (int s = 0; s < 4; ++s)
#pragma unroll
          for (int t = 0; t < 4; ++t) acc[t] = mfma(Wo[t * 4 + s], of[s], acc[t]);
        const f4 lo = sel4(half, acc[2], acc[0]), hi = sel4(half, acc[3], acc[1]);
        st128(rs, (l ? WS_P1 : WS_P0) + poff, v4u{nosent(pk2(lo.x, lo.y)), nosent(pk2(lo.z, lo.w)), nosent(pk2(hi.x, hi.y)), nosent(pk2(hi.z, hi.w))});
      }
      STAMP(7 + l * 8);
    }
  } else if (r < 31) {
    const int mi = xcd * 7 + (r - 24);
    const v2u bb1 = *(const GAS v2u*)(a.b1 + (64 * mi + 32 * (w & 1) + 8 * gq + 4 * half) * 2);
    const unsigned char* g1_ = a.w1 + (size_t)(128 * w) * (FF * 2) + mi * 128;
    const unsigned char* g2_ = a.w2 + (size_t)(64 * mi + 8 * w) * 2048;
    const u32 ldsw = lds0 + w * 16384;
    lptr Lw = L + w * 16384;
    until(t_start, PACEV(P_M0));
    slab64_issue4<0>(g1_, FF * 2, ldsw, lane); slab64_issue4<1>(g1_, FF * 2, ldsw, lane);
    slab64_issue4<2>(g1_, FF * 2, ldsw, lane); slab64_issue4<3>(g1_, FF * 2, ldsw, lane);
    const u64 tag = epoch(a.ws) + 1;
    STAMP(1);
    v4u W1[16], W2[16];
    vmwait<12>(); slab64_read1<0>(Lw, lane, W1); lgkm0(); rows2k_issue4<0>(g2_, ldsw, lane);
    vmwait<12>(); slab64_read1<1>(Lw, lane, W1); lgkm0(); rows2k_issue4<1>(g2_, ldsw, lane);
    vmwait<12>(); slab64_read1<2>(Lw, lane, W1); lgkm0(); rows2k_issue4<2>(g2_, ldsw, lane);
    vmwait<12>(); slab64_read1<3>(Lw, lane, W1); lgkm0(); rows2k_issue4<3>(g2_, ldsw, lane);
    STAMP(2);
    lin_colsum_partial(L, W1, lane, w);
    bar();
    f4 cs = z;
    if (w < 2) cs = lin_sum(L, lane, w);
    vmwait<0>();
    bar();
    rows2k_read(L, lane, w, W2);
    STAMP(3);
    wait_stage(rs, 0, tag, lane, dead);
    wait_stage(rs, 1, tag, lane, dead);
    const f4 bias1 = {bflo(bb1.x), bfhi(bb1.x), bflo(bb1.y), bfhi(bb1.y)};
    const u32 moff = (u32)((16 * w + 4 * half + gq) * 56 + mi) * 128 + b8 * 16;
    bar();
    u32 T0 = 0;
#pragma unroll 1
    for (int l = 0; l < 2; ++l) {
      if (l) { wait_stage(rs, 3, tag, lane, dead); T0 = wait_t0(rs, dead); until(T0, PACEV(P_X1)); }
      v4u xv[2];
      gather128<2>(rs, WS_X + (2 * l) * X_BYTES + (16 * w + 4 * half + gq) * 128 + b8 * 16, 1024, xv, dead);
      if (l == 0 && mi == PACER && w == 7 && lane == 0) st64(rs, WS_T0, v2u{nosent(now()), 1u});
      STAMP(4 + l * 8);
      lin_partial(L, W1, xv[0], xv[1], lane, w);
      bar();
      if (w < 2) {
        float mean, var;
        const f4 acc = lin_final(L, lane, w, mean, var);
        const float r1 = __builtin_amdgcn_rsqf(var + 1e-5f);
        const float r2 = __builtin_amdgcn_rsqf(var * r1 * r1 + 1e-5f);
        const f4 zz = (acc - mean * cs) * (r1 * r2) + bias1;
        float hv[4];
#pragma unroll
        for (int i = 0; i < 4; ++i) {
          const float t = zz[i];
          const float uu = t * (1.0f + 0.044715f * t * t) * (-2.0f * 0.7978845608028654f * 1.4426950408889634f);
          hv[i] = t * __builtin_amdgcn_rcpf(1.0f + __builtin_amdgcn_exp2f(uu));
        }
        *(LAS v2u*)(L + L_IMG + b8 * 160 + (32 * w + 8 * gq + 4 * half) * 2) = v2u{pk2(hv[0], hv[1]), pk2(hv[2], hv[3])};
      }
      bar();
      STAMP(5 + l * 8);
      {
        const v4u h0 = *(LAS v4u*)(L + L_IMG + b8 * 160 + gq * 16), h1 = *(LAS v4u*)(L + L_IMG + b8 * 160 + 64 + gq * 16);
        sched();
#pragma unroll
        for (int uu = 0; uu < 2; ++uu) {
          f4 acc[4];
#pragma unroll
          for (int t = 0; t < 4; ++t) { acc[t] = mfma(W2[(uu * 4 + t) * 2], h0, z); acc[t] = mfma(W2[(uu * 4 + t) * 2 + 1], h1, acc[t]); }
          const f4 lo = sel4(half, acc[2], acc[0]), hi = sel4(half, acc[3], acc[1]);
          st128(rs, (l ? WS_M1 : WS_M0) + moff + uu * (8 * 56 * 128),
                v4u{nosent(pk2(lo.x, lo.y)), nosent(pk2(lo.z, lo.w)), nosent(pk2(hi.x, hi.y)), nosent(pk2(hi.z, hi.w))});
        }
      }
      STAMP(6 + l * 8);
    }
  } else {
    const v4u sent4 = {SENT, SENT, SENT, SENT};
    const u32 wv = xcd * 8 + w;
    u64 tag = 0;
#define CLEAR_STAGE(S, LO, HI)                                                                                   \
    {                                                                                                              \
      _Pragma("unroll 4") for (u32 o = (LO) + wv * 1024; o < (HI); o += 65536) st128(rs, o + lane * 16, sent4);    \
      if ((S) == 0) tag = epoch(a.ws) + 1;                                                                         \
      vmwait<0>();                                                                                                 \
      bar();                                                                                                       \
      if (tid == 0) st64(rs, WS_F + (S) * 64 + xcd * 8, v2u{(u32)tag, (u32)(tag >> 32)});                          \
      STAMP(1 + (S));                                                                                              \
    }
    CLEAR_STAGE(0, WS_S0, WS_S1)
    CLEAR_STAGE(1, WS_S1, WS_S2)
    until(t_start, PACEV(P_C2));
    CLEAR_STAGE(2, WS_S2, WS_S3)
    CLEAR_STAGE(3, WS_S3, WS_END)
#undef CLEAR_STAGE
  }
}
}

extern "C" void kernel_launch(void* const* d_in, const int* in_sizes, int n_in, void* d_out, int out_size, void* d_ws, size_t ws_size,
                              hipStream_t stream) {
  Args a;
  a.x = (const unsigned char*)d_in[0];
  a.kc = (const unsigned char*)d_in[1];
  a.vc = (const unsigned char*)d_in[2];
  a.wq = (const unsigned char*)d_in[3];
  a.wo = (const unsigned char*)d_in[4];
  a.w1 = (const unsigned char*)d_in[6];
  a.b1 = (const unsigned char*)d_in[7];
  a.w2 = (const unsigned char*)d_in[8];
  a.b2 = (const unsigned char*)d_in[9];
  a.out = (unsigned char*)d_out;
  a.ws = (unsigned char*)d_ws;
  (void)hipFuncSetAttribute((const void*)fused_decode, hipFuncAttributeMaxDynamicSharedMemorySize, LDS_BYTES);
  hipLaunchKernelGGL(fused_decode, dim3(256), dim3(512), LDS_BYTES, stream, a);
}
```

```cpp
#include <hip/hip_runtime.h>
#include <stdint.h>

namespace {
typedef unsigned u32;
typedef unsigned long long u64;
typedef u32 v4u __attribute__((ext_vector_type(4)));
typedef u32 v2u __attribute__((ext_vector_type(2)));
typedef float f4 __attribute__((ext_vector_type(4)));
typedef __bf16 bf8 __attribute__((ext_vector_type(8)));
typedef __bf16 bf2 __attribute__((ext_vector_type(2)));
typedef short s4 __attribute__((ext_vector_type(4)));
#define LAS __attribute__((address_space(3)))
#define GAS __attribute__((address_space(1)))
typedef LAS unsigned char* lptr;
typedef __amdgpu_buffer_rsrc_t rsrc_t;

constexpr int DM = 1024, SK = 4096, FF = 3584;
constexpr u32 SENT = 0xFFFFFFFFu;
constexpr int SC1 = 16;
constexpr u32 SPIN_CAP = 1u << 16;
constexpr int BACKOFF = 2;

constexpr u32 WS_E = 0;
constexpr u32 WS_F1 = 2048;
constexpr u32 WS_F2 = 3072;
constexpr u32 WS_T0 = 1536;
constexpr u32 X_BYTES = 16384, Q_BYTES = 65536, A_ROW = 512, A_BYTES = 32 * 8 * 4 * A_ROW, P_BYTES = 128 * 32 * 128, M_BYTES = 128 * 56 * 128;
constexpr u32 WS_S0 = 8192;
constexpr u32 WS_X = WS_S0;
constexpr u32 WS_R = WS_X + 3 * X_BYTES;
constexpr u32 R_BYTES = 32768;
constexpr u32 WS_Q0 = WS_R + 2 * R_BYTES;
constexpr u32 WS_A0 = WS_Q0 + Q_BYTES;
constexpr u32 WS_P0 = WS_A0 + A_BYTES;
constexpr u32 WS_M0 = WS_P0 + P_BYTES;
constexpr u32 WS_S1E = WS_M0 + M_BYTES;
constexpr u32 WS_S2 = WS_S1E + 65536;
constexpr u32 WS_Q1 = WS_S2, WS_A1 = WS_Q1 + Q_BYTES;
constexpr u32 WS_P1 = WS_A1 + A_BYTES, WS_M1 = WS_P1 + P_BYTES;
constexpr u32 WS_END = WS_M1 + M_BYTES;
static_assert((WS_S1E - WS_S0) % 1024 == 0 && (WS_END - WS_S2) % 1024 == 0, "filled in 1 KB pieces");

constexpr u32 L_WK = 131072;
constexpr u32 L_RED = L_WK, L_STAT = L_WK + 16384, L_IMG = L_WK + 17408, L_CS = L_WK + 19712;
constexpr u32 L_QIMG = L_WK, L_MX = L_WK + 2560, L_LS = L_WK + 3072, L_PIMG = L_WK + 3584;
constexpr u32 L_DUMMY = L_WK + 21760;
constexpr u32 LDS_BYTES = L_WK + 22784;

constexpr u32 P_T0 = 120;
constexpr u32 P_M0 = 430;
constexpr u32 P_M0S = 200;
constexpr u32 P_C2 = 40;
constexpr u32 P_M0W = 165, P_X2 = 246, P_Q1 = 382, P_A1 = 541, P_P1 = 656, P_X1 = 721, P_M1W = 899;
constexpr u32 P_MARG = 15;

__device__ __forceinline__ void bar() { asm volatile("s_waitcnt lgkmcnt(0)\n\ts_barrier" ::: "memory"); }
__device__ __forceinline__ void lgkm0() { asm volatile("s_waitcnt lgkmcnt(0)" ::: "memory"); }
template <int N> __device__ __forceinline__ void vmwait() { asm volatile("s_waitcnt vmcnt(%0)" ::"i"(N) : "memory"); }
__device__ __forceinline__ u32 now() { return (u32)__builtin_amdgcn_s_memrealtime(); }
__device__ __forceinline__ void until(u32 ref, u32 d) {
  if (d) { const u32 t0 = now(); for (u32 t = t0; (int)(t - ref) < (int)d && t - t0 < 2 * d; t = now()) { } }
}

template <u32 GO, u32 LO, u32 XR> __device__ __forceinline__ void dma16(u32 voff, const void* sbase, u32 dst) {
  u32 keep, vt;
  asm volatile("s_mov_b32 %0, m0\n\ts_add_u32 m0, %4, %6\n\tv_xor_b32 %1, %7, %2\n\tv_add_u32 %1, %5, %1\n\ts_nop 0\n\t"
               "global_load_lds_dwordx4 %1, %3 nt\n\ts_mov_b32 m0, %0"
               : "=&s"(keep), "=&v"(vt) : "v"(voff), "s"(sbase), "s"(dst), "i"(GO), "i"(LO), "i"(XR) : "memory", "scc");
}
__device__ __forceinline__ void dma16p(u32 voff, const void* sbase, u32 dst) {
  u32 keep;
  asm volatile("s_mov_b32 %0, m0\n\ts_mov_b32 m0, %3\n\ts_nop 2\n\tglobal_load_lds_dwordx4 %1, %2\n\ts_mov_b32 m0, %0"
               : "=&s"(keep) : "v"(voff), "s"(sbase), "s"(dst) : "memory");
}
__device__ __forceinline__ void touch(const unsigned char* ws, u32 off, u32 dummy) {
  u32 keep;
  asm volatile("s_mov_b32 %0, m0\n\ts_mov_b32 m0, %3\n\ts_nop 2\n\tglobal_load_lds_dwordx4 %1, %2 sc1\n\ts_mov_b32 m0, %0"
               : "=&s"(keep) : "v"(off), "s"(ws), "s"(dummy) : "memory");
}
__device__ __forceinline__ f4 mfma(v4u a, v4u b, f4 c) {
  return __builtin_amdgcn_mfma_f32_16x16x32_bf16(__builtin_bit_cast(bf8, a), __builtin_bit_cast(bf8, b), c, 0, 0, 0);
}
__device__ __forceinline__ v2u trread(lptr p) { return __builtin_bit_cast(v2u, __builtin_amdgcn_ds_read_tr16_b64_v4i16((LAS s4*)p)); }
__device__ __forceinline__ u32 pk2(float a, float b) { bf2 v = {(__bf16)a, (__bf16)b}; return __builtin_bit_cast(u32, v); }
__device__ __forceinline__ u32 nosent(u32 v) { return v < 0xFFFFFFFEu ? v : 0xFFFFFFFEu; }
__device__ __forceinline__ float bflo(u32 v) { return __uint_as_float(v << 16); }
__device__ __forceinline__ float bfhi(u32 v) { return __uint_as_float(v & 0xFFFF0000u); }
__device__ __forceinline__ float rbf(float v) { return bflo(pk2(v, v)); }
__device__ __forceinline__ u32 ror8u(u32 v) { return (u32)__builtin_amdgcn_mov_dpp((int)v, 0x128, 0xf, 0xf, true); }
__device__ __forceinline__ float ror8(float v) { return __uint_as_float(ror8u(__float_as_uint(v))); }
__device__ __forceinline__ float vmax(float a, float b) { float r; asm("v_max_f32 %0, %1, %2" : "=v"(r) : "v"(a), "v"(b)); return r; }
__device__ __forceinline__ float vmax3(float a, float b, float c) { float r; asm("v_max3_f32 %0, %1, %2, %3" : "=v"(r) : "v"(a), "v"(b), "v"(c)); return r; }
__device__ __forceinline__ void sched() { __builtin_amdgcn_sched_barrier(0); }
__device__ __forceinline__ float x16max(float x) { auto s = __builtin_amdgcn_permlane16_swap(__float_as_uint(x), __float_as_uint(x), false, false); return vmax(__uint_as_float(s[0]), __uint_as_float(s[1])); }
__device__ __forceinline__ float x32max(float x) { auto s = __builtin_amdgcn_permlane32_swap(__float_as_uint(x), __float_as_uint(x), false, false); return vmax(__uint_as_float(s[0]), __uint_as_float(s[1])); }
__device__ __forceinline__ float x16sum(float x) { auto s = __builtin_amdgcn_permlane16_swap(__float_as_uint(x), __float_as_uint(x), false, false); return __uint_as_float(s[0]) + __uint_as_float(s[1]); }
__device__ __forceinline__ float x32sum(float x) { auto s = __builtin_amdgcn_permlane32_swap(__float_as_uint(x), __float_as_uint(x), false, false); return __uint_as_float(s[0]) + __uint_as_float(s[1]); }
__device__ __forceinline__ f4 sel4(bool c, f4 t, f4 f) { f4 r; r.x = c ? t.x : f.x; r.y = c ? t.y : f.y; r.z = c ? t.z : f.z; r.w = c ? t.w : f.w; return r; }
__device__ __forceinline__ u32 umax(u32 a, u32 b) { return a > b ? a : b; }
__device__ __forceinline__ u32 max4(v4u v) { return umax(umax(v.x, v.y), umax(v.z, v.w)); }
__device__ __forceinline__ v4u ld128(rsrc_t rs, u32 off, u32 so = 0) { return __builtin_amdgcn_raw_buffer_load_b128(rs, off, so, SC1); }
__device__ __forceinline__ v2u ld64(rsrc_t rs, u32 off, u32 so = 0) { return __builtin_amdgcn_raw_buffer_load_b64(rs, off, so, SC1); }
__device__ __forceinline__ void st128(rsrc_t rs, u32 off, u32 so, v4u v) { __builtin_amdgcn_raw_buffer_store_b128(v, rs, off + so, 0, SC1); }
__device__ __forceinline__ void st64(rsrc_t rs, u32 off, u32 so, v2u v) { __builtin_amdgcn_raw_buffer_store_b64(v, rs, off, so, SC1); }

__device__ __forceinline__ u64 epoch(const unsigned char* ws) {
  u64 e;
  asm volatile("s_load_dwordx2 %0, %1, 0x0\n\ts_waitcnt lgkmcnt(0)" : "=&s"(e) : "s"(ws) : "memory");
  return e;
}
__device__ __forceinline__ void reanchor(u32& T0, u32 next_read, u32 margin) {
  const int late = (int)(now() - T0) - ((int)next_read - 10);
  if (late > 3 && late < 2000) T0 += late + margin;
}
__device__ __forceinline__ void wait_stage(rsrc_t rs, int stage, u64 tag, int lane, bool& dead) {
  const u32 lo = (u32)tag, hi = (u32)(tag >> 32);
  for (u32 spins = 0; !dead; ++spins) {
    asm volatile("" ::: "memory");
    bool ok;
    if (stage == 0) { const v2u v = ld64(rs, (lane < 56 ? lane : 55) * 8, WS_F1); ok = v.x == lo && v.y == hi; }
    else { const v4u v = ld128(rs, lane * 16, WS_F2); ok = v.x == lo && v.y == hi && v.z == lo && v.w == hi; }
    if (__all(ok)) break;
    if (spins > SPIN_CAP) dead = true;
    __builtin_amdgcn_s_sleep(BACKOFF);
  }
}
__device__ __forceinline__ u32 wait_t0(rsrc_t rs, u64 tag, bool& dead) {
  v2u v = {0u, 0u};
  for (u32 spins = 0; !dead; ++spins) {
    asm volatile("" ::: "memory");
    v = ld64(rs, 0, WS_T0);
    if (__all(v.y == (u32)tag)) break;
    if (spins > SPIN_CAP) dead = true;
    __builtin_amdgcn_s_sleep(BACKOFF);
  }
  return __builtin_amdgcn_readfirstlane(v.x);
}
template <int N> __device__ __forceinline__ void gather128(rsrc_t rs, u32 off, u32 so, u32 stride, v4u (&v)[N], bool& dead) {
  for (u32 spins = 0;; ++spins) {
    asm volatile("" ::: "memory");
#pragma unroll
    for (int i = 0; i < N; ++i) v[i] = ld128(rs, off, so + i * stride);
    u32 m = max4(v[0]);
#pragma unroll
    for (int i = 1; i < N; ++i) m = umax(m, max4(v[i]));
    if (__all(m != SENT) || dead) break;
    if (spins > SPIN_CAP) dead = true;
    __builtin_amdgcn_s_sleep(BACKOFF);
  }
}

template <int G, u32 ROWB> __device__ __forceinline__ void slab64_issue4(const unsigned char* gbase, u32 ldsw, int lane) {
  const u32 voff = (u32)(lane >> 3) * ROWB + (u32)(((lane & 7) ^ (((lane >> 4) & 1) << 2)) * 16);
  dma16<(4 * G + 0) * 8 * ROWB, (4 * G + 0) * 1024, 0>(voff, gbase, ldsw);
  dma16<(4 * G + 1) * 8 * ROWB, (4 * G + 1) * 1024, 0>(voff, gbase, ldsw);
  dma16<(4 * G + 2) * 8 * ROWB, (4 * G + 2) * 1024, 0>(voff, gbase, ldsw);
  dma16<(4 * G + 3) * 8 * ROWB, (4 * G + 3) * 1024, 0>(voff, gbase, ldsw);
}
template <int S> __device__ __forceinline__ void slab64_read1(lptr Lw, int lane, v4u (&A)[16]) {
  const int gq = lane >> 4, q = (lane >> 2) & 3, p = lane & 3, t = q >> 1;
  lptr a0 = Lw + (8 * gq + q) * 128 + (4 * t + p) * 16, a1 = Lw + (8 * gq + q) * 128 + (4 * (1 - t) + p) * 16;
#pragma unroll
  for (int u = 0; u < 2; ++u)
#pragma unroll
    for (int hf = 0; hf < 2; ++hf) {
      lptr a = (u ? a1 : a0) + S * 4096 + 8 * hf;
      const v2u lo = trread(a), hi = trread(a + 512);
      A[(u * 2 + hf) * 4 + S] = v4u{lo.x, lo.y, hi.x, hi.y};
    }
}
template <int G> __device__ __forceinline__ void rows1k_issue4(const unsigned char* gbase, u32 ldsw, int lane) {
  dma16<(32 * G + 0) * 2048, (32 * G + 0) * 1024, 0>(lane * 16, gbase, ldsw);
  dma16<(32 * G + 1) * 2048, (32 * G + 1) * 1024, 64>(lane * 16, gbase, ldsw);
  dma16<(32 * G + 2) * 2048, (32 * G + 2) * 1024, 128>(lane * 16, gbase, ldsw);
  dma16<(32 * G + 3) * 2048, (32 * G + 3) * 1024, 192>(lane * 16, gbase, ldsw);
}
template <int S> __device__ __forceinline__ void rows1k_read(lptr L, int lane, int w, v4u (&A)[16]) {
  const int gq = lane >> 4, q = (lane >> 2) & 3, p = lane & 3;
#pragma unroll
  for (int u = 0; u < 2; ++u) {
    lptr ad = L + S * 32768 + (8 * gq + q) * 1024 + (((8 * w + 4 * u) ^ (4 * q)) + p) * 16;
#pragma unroll
    for (int hf = 0; hf < 2; ++hf) {
      const v2u lo = trread(ad + 8 * hf), hi = trread(ad + 4096 + 8 * hf);
      A[(u * 2 + hf) * 4 + S] = v4u{lo.x, lo.y, hi.x, hi.y};
    }
  }
}
template <int G> __device__ __forceinline__ void rows2k_issue4(const unsigned char* gbase, u32 ldsw, int lane) {
  dma16<(4 * G + 0) * 1024, (4 * G + 0) * 1024, ((2 * G + 0) & 3) * 64>(lane * 16, gbase, ldsw);
  dma16<(4 * G + 1) * 1024, (4 * G + 1) * 1024, ((2 * G + 0) & 3) * 64>(lane * 16, gbase, ldsw);
  dma16<(4 * G + 2) * 1024, (4 * G + 2) * 1024, ((2 * G + 1) & 3) * 64>(lane * 16, gbase, ldsw);
  dma16<(4 * G + 3) * 1024, (4 * G + 3) * 1024, ((2 * G + 1) & 3) * 64>(lane * 16, gbase, ldsw);
}
__device__ __forceinline__ void rows2k_read(lptr L, int lane, int w, v4u (&A)[16]) {
  const int gq = lane >> 4, q = (lane >> 2) & 3, p = lane & 3;
#pragma unroll
  for (int u = 0; u < 4; ++u) {
    lptr ad = L + (8 * gq + q) * 2048 + (16 * w + 4 * (u ^ q) + p) * 16;
#pragma unroll
    for (int hf = 0; hf < 2; ++hf)
#pragma unroll
      for (int s = 0; s < 2; ++s) {
        const v2u lo = trread(ad + s * 65536 + 8 * hf), hi = trread(ad + s * 65536 + 8192 + 8 * hf);
        A[(u * 2 + hf) * 2 + s] = v4u{lo.x, lo.y, hi.x, hi.y};
      }
  }
}

__device__ __forceinline__ void row_stats_partial(lptr L, u32 base, f4 G, f4 S, int lane, int w) {
  const int n = lane & 15;
  const float g01 = (lane & 1) ? G.y : G.x, g23 = (lane & 1) ? G.w : G.z, g = (lane & 2) ? g23 : g01;
  if ((lane >> 4) == (n >> 2)) *(LAS v2u*)(L + base + n * 64 + w * 8) = v2u{__float_as_uint(S.x), __float_as_uint(g)};
}
__device__ __forceinline__ void row_stats(f4 (&t)[4], float& mean, float& var) {
  float s1 = ((t[0].x + t[0].z) + (t[1].x + t[1].z)) + ((t[2].x + t[2].z) + (t[3].x + t[3].z));
  float s2 = ((t[0].y + t[0].w) + (t[1].y + t[1].w)) + ((t[2].y + t[2].w) + (t[3].y + t[3].w));
  s1 += ror8(s1); s2 += ror8(s2);
  mean = s1 * (1.0f / DM);
  var = fmaxf(s2 * (1.0f / DM) - mean * mean, 0.f);
}
__device__ __forceinline__ void lin_partial(lptr L, const v4u (&A)[16], v4u X0, v4u X1, int lane, int w) {
  const f4 z = {0.f, 0.f, 0.f, 0.f};
  const v4u ones = {0x3F803F80u, 0x3F803F80u, 0x3F803F80u, 0x3F803F80u};
  f4 G = mfma(X0, X0, z), S = mfma(ones, X0, z);
  G = mfma(X1, X1, G); S = mfma(ones, X1, S);
  const v4u R0 = {ror8u(X0.x), ror8u(X0.y), ror8u(X0.z), ror8u(X0.w)}, R1 = {ror8u(X1.x), ror8u(X1.y), ror8u(X1.z), ror8u(X1.w)};
  __builtin_amdgcn_sched_barrier(0);
  f4 acc[4];
#pragma unroll
  for (int u = 0; u < 2; ++u) {
    f4 a = mfma(A[(u * 2) * 4 + 0], X0, z), c = mfma(A[(u * 2 + 1) * 4 + 1], X0, z);
    a = mfma(A[(u * 2) * 4 + 2], X1, a); c = mfma(A[(u * 2 + 1) * 4 + 3], X1, c);
    a = mfma(A[(u * 2) * 4 + 1], R0, a); c = mfma(A[(u * 2 + 1) * 4 + 0], R0, c);
    a = mfma(A[(u * 2) * 4 + 3], R1, a); c = mfma(A[(u * 2 + 1) * 4 + 2], R1, c);
    acc[u * 2] = a; acc[u * 2 + 1] = c;
    if (u == 0) {
      row_stats_partial(L, L_STAT, G, S, lane, w);
    }
  }
  const bool half = (lane >> 3) & 1;
#pragma unroll
  for (int u = 0; u < 2; ++u) *(LAS f4*)(L + L_RED + (w * 2 + u) * 1024 + lane * 16) = sel4(half, acc[u * 2 + 1], acc[u * 2]);
}
__device__ __forceinline__ void lin_colsum_partial(lptr L, const v4u (&A)[16], int lane, int w) {
  const f4 z = {0.f, 0.f, 0.f, 0.f};
  const v4u ones = {0x3F803F80u, 0x3F803F80u, 0x3F803F80u, 0x3F803F80u};
  const bool half = (lane >> 3) & 1;
#pragma unroll
  for (int u = 0; u < 2; ++u) {
    f4 a = z, c = z;
#pragma unroll
    for (int s = 0; s < 4; ++s) { a = mfma(A[(u * 2) * 4 + s], ones, a); c = mfma(A[(u * 2 + 1) * 4 + s], ones, c); }
    if ((lane & 7) == 0) *(LAS f4*)(L + L_CS + (w * 2 + u) * 128 + (half * 4 + (lane >> 4)) * 16) = sel4(half, c, a);
  }
}
__device__ __forceinline__ f4 lin_colsum(lptr L, int lane, int u) {
  f4 v[8];
#pragma unroll
  for (int i = 0; i < 8; ++i) v[i] = *(LAS f4*)(L + L_CS + (i * 2 + u) * 128 + (((lane >> 3) & 1) * 4 + (lane >> 4)) * 16);
  return ((v[0] + v[1]) + (v[2] + v[3])) + ((v[4] + v[5]) + (v[6] + v[7]));
}
__device__ __forceinline__ f4 lin_final(lptr L, int lane, int u, float& mean, float& var) {
  f4 t[4], v[8];
#pragma unroll
  for (int i = 0; i < 4; ++i) t[i] = *(LAS f4*)(L + L_STAT + (lane & 15) * 64 + i * 16);
#pragma unroll
  for (int i = 0; i < 8; ++i) v[i] = *(LAS f4*)(L + L_RED + (i * 2 + u) * 1024 + lane * 16);
  sched();
  row_stats(t, mean, var);
  return ((v[0] + v[1]) + (v[2] + v[3])) + ((v[4] + v[5]) + (v[6] + v[7]));
}

__device__ __forceinline__ void ln_input(lptr L, int lane, int w, v4u& X0, v4u& X1) {
  const f4 z = {0.f, 0.f, 0.f, 0.f};
  const v4u ones = {0x3F803F80u, 0x3F803F80u, 0x3F803F80u, 0x3F803F80u};
  X0 = *(LAS v4u*)(L + L_RED + w * 2048 + lane * 16); X1 = *(LAS v4u*)(L + L_RED + w * 2048 + 1024 + lane * 16);
  f4 G = mfma(X0, X0, z), S = mfma(ones, X0, z);
  G = mfma(X1, X1, G); S = mfma(ones, X1, S);
  row_stats_partial(L, L_IMG, G, S, lane, w);
  bar();
  f4 t[4];
#pragma unroll
  for (int i = 0; i < 4; ++i) t[i] = *(LAS f4*)(L + L_IMG + (lane & 15) * 64 + i * 16);
  float mean, var;
  row_stats(t, mean, var);
  const float mb = rbf(mean), inv = __builtin_amdgcn_rcpf(rbf(__builtin_amdgcn_sqrtf(rbf(rbf(var) + 1e-5f))));
#define LN2(v) pk2(rbf(bflo(v) - mb) * inv, rbf(bfhi(v) - mb) * inv)
  X0 = v4u{LN2(X0.x), LN2(X0.y), LN2(X0.z), LN2(X0.w)};
  X1 = v4u{LN2(X1.x), LN2(X1.y), LN2(X1.z), LN2(X1.w)};
  asm volatile("" : "+v"(X0), "+v"(X1));
#undef LN2
}
__device__ __forceinline__ void q_publish(lptr L, rsrc_t rs, int lane, int w, int l, f4 cs, bool cut, u32 voff, u32 soff) {
  float mean, var;
  const f4 acc = lin_final(L, lane, w, mean, var);
  float rstd = __builtin_amdgcn_rcpf(rbf(__builtin_amdgcn_sqrtf(rbf(rbf(var) + 1e-5f))));
  mean = rbf(mean);
  if (l == 0) { mean = 0.f; rstd = 1.f; }
  const f4 qv = (acc - mean * cs) * rstd;
  if (cut) {
    st128(rs, voff, soff, v4u{nosent(__float_as_uint(qv.x)), nosent(__float_as_uint(qv.y)), nosent(__float_as_uint(qv.z)), nosent(__float_as_uint(qv.w))});
  } else {
    const u32 d0 = nosent(pk2(qv.x, qv.y)), d1 = nosent(pk2(qv.z, qv.w));
    const u32 e0 = ror8u(d0), e1 = ror8u(d1);
    if (!((lane >> 3) & 1)) st128(rs, voff, soff, v4u{d0, d1, e0, e1});
  }
}

struct Args {
  const unsigned char *x, *kc, *vc, *wq, *wo, *w1, *b1, *w2, *b2;
  unsigned char *out, *ws;
};

__global__ void __launch_bounds__(512, 2) fused_decode(Args a) {
  extern __shared__ __attribute__((aligned(16))) unsigned char smem[];
  lptr L = (lptr)smem;
  const u32 lds0 = (u32)(uintptr_t)L;
  const int tid = threadIdx.x, lane = tid & 63, w = __builtin_amdgcn_readfirstlane(tid >> 6);
  const int c = blockIdx.x, xcd = c & 7, r = c >> 3;
  const rsrc_t rs = __builtin_amdgcn_make_buffer_rsrc((void*)a.ws, 0, WS_END, 0x00020000);
  const int gq = lane >> 4, half = (lane >> 3) & 1, b8 = lane & 7;
  const f4 z = {0.f, 0.f, 0.f, 0.f};
  bool dead = false;
  const u32 t_start = now();
  const int slow = (3 - (int)((uintptr_t)a.wq >> 7)) & 7;

  if (w == 7 && r < 16) touch(a.out, (lane & 3) * 4096 + xcd * 256, lds0 + L_DUMMY);
  if (w == 6 && r < 16) {
    const size_t kv0 = (size_t)((xcd * 4 + (r >> 2)) * SK + (SK - 1024) + 256 * (r & 3)) * 256;
    touch(a.kc + kv0, (lane & 3) * 16384, lds0 + L_DUMMY);
    touch(a.vc + kv0, (lane & 3) * 16384, lds0 + L_DUMMY);
  }
  if (w == 7 && r >= 16 && r < 24) touch(a.wo + (size_t)(128 * ((xcd * 8 + r - 16) >> 1)) * 2048, (lane & 3) * 65536, lds0 + L_DUMMY);
  if (w == 7 && r >= 24 && r < 31) {
    touch(a.w2 + (size_t)(64 * (xcd * 7 + r - 24)) * 2048, (lane & 1) * 65536, lds0 + L_DUMMY);
    touch(a.w1, (lane & 7) * (128 * FF * 2) + (xcd * 7 + r - 24) * 128, lds0 + L_DUMMY);
  }
  if (r < 16) {
    until(t_start, P_T0);
    const size_t kvrow = (size_t)((xcd * 4 + (r >> 2)) * SK + (SK - 1024) + 256 * (r & 3) + 32 * w) * 256;
    const u32 rr = lane >> 4, cs = lane & 15;
    const u32 vk = rr * 256 + ((cs ^ rr) * 16), vv = rr * 256 + ((cs ^ (rr << 1)) * 16);
    const unsigned char *kb = a.kc + kvrow, *vb = a.vc + kvrow;
    const u32 kd = lds0 + w * 8192, vd = lds0 + 65536 + w * 8192;
    dma16<0, 0, 0>(vk, kb, kd); dma16<1024, 1024, 64>(vk, kb, kd); dma16<2048, 2048, 128>(vk, kb, kd); dma16<3072, 3072, 192>(vk, kb, kd);
    dma16<4096, 4096, 0>(vk, kb, kd); dma16<5120, 5120, 64>(vk, kb, kd); dma16<6144, 6144, 128>(vk, kb, kd); dma16<7168, 7168, 192>(vk, kb, kd);
    dma16<0, 0, 0>(vv, vb, vd); dma16<1024, 1024, 128>(vv, vb, vd); dma16<2048, 2048, 0>(vv, vb, vd); dma16<3072, 3072, 128>(vv, vb, vd);
    dma16<4096, 4096, 0>(vv, vb, vd); dma16<5120, 5120, 128>(vv, vb, vd); dma16<6144, 6144, 0>(vv, vb, vd); dma16<7168, 7168, 128>(vv, vb, vd);
  } else if (r < 24 || r == 31) {
    const int qb = xcd * 8 + (r == 31 ? slow : r - 16);
    const u32 vx = b8 * (DM * 2) + (128 * w + 32 * half + 8 * gq) * 2;
    dma16p(vx, a.x, lds0 + L_RED + w * 2048);
    dma16p(vx + 128, a.x, lds0 + L_RED + w * 2048 + 1024);
    if ((qb & 7) != slow || (r == 31 ? w >= 4 : w < 4)) {
      const unsigned char* gq_ = a.wq + (size_t)(128 * w) * 8192 + qb * 128;
      const u32 ldsw = lds0 + w * 16384;
      slab64_issue4<0, 8192>(gq_, ldsw, lane); slab64_issue4<1, 8192>(gq_, ldsw, lane);
      slab64_issue4<2, 8192>(gq_, ldsw, lane); slab64_issue4<3, 8192>(gq_, ldsw, lane);
    }
  } else {
    u32 sv = SENT; asm volatile("" : "+v"(sv));
    const v4u sent4 = {sv, sv, sv, sv};
#pragma unroll 1
    for (u32 o = WS_S0 + ((xcd * 7 + r - 24) * 8 + w) * 1024; o < WS_S1E; o += 448 * 1024) st128(rs, lane * 16, o, sent4);
  }
  asm volatile("" ::: "memory");
  if (r < 16) {
    const int b = xcd, g = r >> 2, sp = r & 3, j = xcd * 16 + r;
    const u32 colb = (u32)(8 * j + 4 * (gq & 1)) * 2;
    const v2u x0 = *(const GAS v2u*)(a.x + b8 * (DM * 2) + colb), bb2 = *(const GAS v2u*)(a.b2 + colb);
    const u64 tag = epoch(a.ws) + 1;
    v4u Kf[8], Vf[8];
    vmwait<8>();
#pragma unroll
    for (int tk = 0; tk < 2; ++tk)
#pragma unroll
      for (int s = 0; s < 4; ++s)
        Kf[tk * 4 + s] = *(LAS v4u*)(L + w * 8192 + tk * 4096 + (lane & 15) * 256 + (((gq ^ (lane & 15)) ^ (4 * s)) * 16));
    wait_stage(rs, 0, tag, lane, dead);
    vmwait<0>();
    bar();
    {
      const int q = (lane >> 2) & 3, p = lane & 3;
      lptr vb = L + 65536 + (4 * gq + q) * 256 + (((2 * w + (p >> 1)) ^ ((4 * (gq & 1) + q) << 1)) * 16) + 8 * (p & 1);
#pragma unroll
      for (int ks = 0; ks < 8; ++ks) {
        const v2u lo = trread(vb + ks * 8192), hi = trread(vb + ks * 8192 + 4096);
        Vf[ks] = v4u{lo.x, lo.y, hi.x, hi.y};
      }
    }
    f4 xres = {bflo(x0.x), bfhi(x0.x), bflo(x0.y), bfhi(x0.y)};
    const f4 bias2 = {bflo(bb2.x), bfhi(bb2.x), bflo(bb2.y), bfhi(bb2.y)};
    v4u sel;
    {
      const int n = lane & 15;
      sel.x = n == 0 ? 0x00003F80u : n == 1 ? 0x3F800000u : 0u;
      sel.y = n == 2 ? 0x00003F80u : n == 3 ? 0x3F800000u : 0u;
      sel.z = n == 4 ? 0x00003F80u : n == 5 ? 0x3F800000u : 0u;
      sel.w = n == 6 ? 0x00003F80u : n == 7 ? 0x3F800000u : 0u;
    }
    const u32 arow = (u32)(((g * 8 + b8) * 8 + b) * 4 + sp) * A_ROW + (16 * w + 4 * gq) * 4;
    const bool qsplit = (w & 3) == (slow >> 1);
    const bool qmine = ((lane >> 3) & 1) == (slow & 1);
    u32 tref = 0, T0 = 0;
#pragma unroll 1
    for (int l = 0; l < 2; ++l) {
      if (l) { wait_stage(rs, 1, tag, lane, dead); until(T0, P_Q1); }
      v4u qv[1];
      {
        const u32 qo = (l ? WS_Q1 : WS_Q0) + b * 8192 + g * 2048 + w * 256;
        if (!qsplit) gather128<1>(rs, (lane & 15) * 16, qo, 0, qv, dead);
        else {
          const u32 ro = WS_R + l * R_BYTES + (b * 8 + 2 * g + (w >> 2)) * 256;
          v4u p, t0, t1, t2, t3;
          for (u32 spins = 0;; ++spins) {
            asm volatile("" ::: "memory");
            p = ld128(rs, (lane & 15) * 16, qo);
            t0 = ld128(rs, (lane & 7) * 32, ro); t1 = ld128(rs, (lane & 7) * 32 + 16, ro);
            t2 = ld128(rs, (lane & 7) * 32, ro + R_BYTES / 2); t3 = ld128(rs, (lane & 7) * 32 + 16, ro + R_BYTES / 2);
            if (__all(umax(umax(qmine ? 0u : max4(p), umax(max4(t0), max4(t1))), umax(max4(t2), max4(t3))) != SENT) || dead) break;
            if (spins > SPIN_CAP) dead = true;
            __builtin_amdgcn_s_sleep(BACKOFF);
          }
          const f4 lo = __builtin_bit_cast(f4, t0) + __builtin_bit_cast(f4, t2), hi = __builtin_bit_cast(f4, t1) + __builtin_bit_cast(f4, t3);
          qv[0] = v4u{qmine ? pk2(lo.x, lo.y) : p.x, qmine ? pk2(lo.z, lo.w) : p.y, qmine ? pk2(hi.x, hi.y) : p.z, qmine ? pk2(hi.z, hi.w) : p.w};
        }
      }
      if (lane < 16) *(LAS v4u*)(L + L_QIMG + w * 288 + lane * 16) = qv[0];
      bar();
      f4 s0 = z, s1 = z;
      {
        v4u qf[4];
#pragma unroll
        for (int s = 0; s < 4; ++s) qf[s] = *(LAS v4u*)(L + L_QIMG + b8 * 288 + s * 64 + gq * 16);
        sched();
#pragma unroll
        for (int s = 0; s < 4; ++s) { s0 = mfma(Kf[s], qf[s], s0); s1 = mfma(Kf[4 + s], qf[s], s1); }
      }
      f4 sc = sel4(half, s1, s0);
      { const u32 r0 = pk2(sc.x, sc.y), r1 = pk2(sc.z, sc.w); sc = f4{bflo(r0), bfhi(r0), bflo(r1), bfhi(r1)} * 0.08838834764831845f; }
      { const u32 r0 = pk2(sc.x, sc.y), r1 = pk2(sc.z, sc.w); sc = f4{bflo(r0), bfhi(r0), bflo(r1), bfhi(r1)} * 1.4426950408889634f; }
      float mx = vmax3(vmax(sc.x, sc.y), sc.z, sc.w);
      mx = vmax(mx, ror8(mx)); mx = x16max(mx); mx = x32max(mx);
      if (lane < 8) *(LAS float*)(L + L_MX + lane * 32 + w * 4) = mx;
      bar();
      {
        const f4 m0 = *(LAS f4*)(L + L_MX + b8 * 32), m1 = *(LAS f4*)(L + L_MX + b8 * 32 + 16);
        mx = vmax3(vmax3(m0.x, m0.y, m0.z), vmax3(m0.w, m1.x, m1.y), vmax(m1.z, m1.w));
      }
      const float p0 = __builtin_amdgcn_exp2f(sc.x - mx), p1 = __builtin_amdgcn_exp2f(sc.y - mx), p2 = __builtin_amdgcn_exp2f(sc.z - mx), p3 = __builtin_amdgcn_exp2f(sc.w - mx);
      *(LAS v2u*)(L + L_PIMG + ((w * 4 + gq) * 8 + b8) * 16 + half * 8) = v2u{pk2(p0, p1), pk2(p2, p3)};
      float ls = (p0 + p1) + (p2 + p3);
      ls += ror8(ls); ls = x16sum(ls); ls = x32sum(ls);
      if (lane < 8) *(LAS float*)(L + L_LS + lane * 32 + w * 4) = ls;
      bar();
      f4 o0 = z, o1 = z;
      {
        v4u pf[8];
#pragma unroll
        for (int ks = 0; ks < 8; ++ks) pf[ks] = *(LAS v4u*)(L + L_PIMG + ((ks * 4 + gq) * 8 + b8) * 16);
        const f4 l0 = *(LAS f4*)(L + L_LS + b8 * 32), l1 = *(LAS f4*)(L + L_LS + b8 * 32 + 16);
        sched();
#pragma unroll
        for (int ks = 0; ks < 8; ks += 2) { o0 = mfma(Vf[ks], pf[ks], o0); o1 = mfma(Vf[ks + 1], pf[ks + 1], o1); }
        const float tot = ((l0.x + l0.y) + (l0.z + l0.w)) + ((l1.x + l1.y) + (l1.z + l1.w));
        const f4 o = (o0 + o1) * __builtin_amdgcn_rcpf(tot);
        const u32 lse = nosent(__float_as_uint(mx + __builtin_amdgcn_logf(tot)));
        if (!half) st128(rs, arow, l ? WS_A1 : WS_A0, v4u{nosent(pk2(o.x, o.y)), lse, nosent(pk2(o.z, o.w)), lse});
      }
      if (l) reanchor(T0, P_A1, P_MARG); else if (w == 1) tref = now();
      if (w == 0) {
        {
          if (l) until(T0, P_P1);
          v4u pv[4];
          gather128<4>(rs, lane * 16, (l ? WS_P1 : WS_P0) + j * 4096, 1024, pv, dead);
          f4 acc = mfma(sel, pv[0], z), acc2 = mfma(sel, pv[1], z);
          acc = mfma(sel, pv[2], acc); acc2 = mfma(sel, pv[3], acc2);
          acc += acc2;
          xres.x += acc.x + ror8(acc.x); xres.y += acc.y + ror8(acc.y); xres.z += acc.z + ror8(acc.z); xres.w += acc.w + ror8(acc.w);
          if (lane < 32 && !half) st64(rs, b8 * 16 + gq * 8, WS_X + (2 * l) * X_BYTES + j * 128, v2u{nosent(pk2(xres.x, xres.y)), nosent(pk2(xres.z, xres.w))});
          if (l) reanchor(T0, P_X1, P_MARG);
        }
        {
          if (l == 0) T0 = wait_t0(rs, tag, dead);
          until(T0, l ? P_M1W : P_M0W);
          v4u pv[7];
          gather128<7>(rs, lane * 16, (l ? WS_M1 : WS_M0) + j * 7168, 1024, pv, dead);
          f4 acc = mfma(sel, pv[0], z), acc2 = mfma(sel, pv[1], z);
          acc = mfma(sel, pv[2], acc); acc2 = mfma(sel, pv[3], acc2);
          acc = mfma(sel, pv[4], acc); acc2 = mfma(sel, pv[5], acc2);
          acc = mfma(sel, pv[6], acc);
          acc += acc2;
          xres.x += (acc.x + ror8(acc.x)) + bias2.x; xres.y += (acc.y + ror8(acc.y)) + bias2.y;
          xres.z += (acc.z + ror8(acc.z)) + bias2.z; xres.w += (acc.w + ror8(acc.w)) + bias2.w;
          const v2u o = {pk2(xres.x, xres.y), pk2(xres.z, xres.w)};
          if (lane < 32 && !half) {
            if (l == 0) st64(rs, b8 * 16 + gq * 8, WS_X + X_BYTES + j * 128, v2u{nosent(o.x), nosent(o.y)});
            else __hip_atomic_store((GAS u64*)(a.out + b8 * (DM * 2) + (8 * j + 4 * gq) * 2), (u64)o.x | ((u64)o.y << 32), __ATOMIC_RELAXED, __HIP_MEMORY_SCOPE_AGENT);
          }
          if (l == 0) { reanchor(T0, P_X2, P_MARG); if (lane == 0) *(LAS u32*)(L + L_MX + 256) = T0; }
        }
      }
      if (l == 0) {
        if (w == 1) {
          until(tref, P_C2);
          u32 sv = SENT; asm volatile("" : "+v"(sv));
          const v4u sent4 = {sv, sv, sv, sv};
#pragma unroll 1
          for (u32 o = WS_S2 + j * 1024; o < WS_END; o += 128 * 1024) st128(rs, lane * 16, o, sent4);
          vmwait<0>();
          if (lane == 0) st64(rs, 0, WS_F2 + j * 8, v2u{(u32)tag, (u32)(tag >> 32)});
        }
        bar();
        T0 = __builtin_amdgcn_readfirstlane(*(LAS u32*)(L + L_MX + 256));
      }
    }
    if (c == 0 && tid == 0) st64(rs, 0, WS_E, v2u{(u32)tag, (u32)(tag >> 32)});
  } else if (r < 24) {
    const int qi = xcd * 8 + (r - 16), h = qi >> 1, ch = qi & 1;
    const bool loadw = (qi & 7) != slow || w < 4;
    const unsigned char* go_ = a.wo + (size_t)(128 * h + 4 * w) * 2048 + ch * 1024;
    lptr Lw = L + w * 16384;
    const u64 tag = epoch(a.ws) + 1;
    if (loadw) vmwait<16>(); else vmwait<0>();
    v4u X0, X1;
    ln_input(L, lane, w, X0, X1);
    v4u Wq[16], Wo[16];
    if (loadw) {
      vmwait<12>(); slab64_read1<0>(Lw, lane, Wq);
      vmwait<8>();  slab64_read1<1>(Lw, lane, Wq);
      vmwait<4>();  slab64_read1<2>(Lw, lane, Wq);
    } else {
#pragma unroll
      for (int i = 0; i < 16; ++i) Wq[i] = v4u{0u, 0u, 0u, 0u};
    }
    const bool cut = (qi & 7) == slow;
    const u32 qv_ = cut ? b8 * 2048 + (8 * gq + 4 * half) * 4 : b8 * 8192 + gq * 16;
    const u32 qs0 = cut ? WS_R + xcd * 256 + (w & 1) * 128 : WS_Q0 + qi * 128 + (w & 1) * 64, qstep = cut ? R_BYTES : WS_Q1 - WS_Q0;
    const u32 pv_ = (u32)(4 * half + gq) * 4096 + b8 * 16, ps_ = (u32)((64 * ch + 8 * w) * 32 + h) * 128;
    f4 cs = z;
    u32 T0 = 0;
#pragma unroll 1
    for (int l = 0; l < 2; ++l) {
      if (l) {
        wait_stage(rs, 1, tag, lane, dead);
        T0 = wait_t0(rs, tag, dead);
        until(T0, P_X2);
        v4u xv[2];
        gather128<2>(rs, (4 * half + gq) * 128 + b8 * 16, WS_X + X_BYTES + w * 2048, 1024, xv, dead);
        X0 = xv[0]; X1 = xv[1];
      }
      else if (loadw) {
        if (w < 2) wait_stage(rs, 0, tag, lane, dead);
        vmwait<0>();
        slab64_read1<3>(Lw, lane, Wq);
      }
      lin_partial(L, Wq, X0, X1, lane, w);
      bar();
      if (w < 2) q_publish(L, rs, lane, w, l, cs, cut, qv_, qs0 + l * qstep);
      if (l) reanchor(T0, P_Q1, P_MARG);
      if (l == 0) {
        bar();
        rows1k_issue4<0>(go_, lds0 + w * 4096, lane); rows1k_issue4<1>(go_, lds0 + w * 4096, lane);
        rows1k_issue4<2>(go_, lds0 + w * 4096, lane); rows1k_issue4<3>(go_, lds0 + w * 4096, lane);
        lin_colsum_partial(L, Wq, lane, w);
        vmwait<0>(); bar();
        rows1k_read<0>(L, lane, w, Wo); rows1k_read<1>(L, lane, w, Wo); sched();
        rows1k_read<2>(L, lane, w, Wo); rows1k_read<3>(L, lane, w, Wo);
        if (w < 2) cs = lin_colsum(L, lane, w);
      }
      if (l) until(T0, P_A1);
      {
        const u32 row = (l ? WS_A1 : WS_A0) + (u32)((h * 8 + w) * 4) * A_ROW;
        v2u ov[4];
        for (u32 spins = 0;; ++spins) {
          asm volatile("" ::: "memory");
#pragma unroll
          for (int s = 0; s < 4; ++s) ov[s] = ld64(rs, lane * 8, row + s * A_ROW);
          u32 m = umax(ov[0].x, ov[0].y);
#pragma unroll
          for (int s = 1; s < 4; ++s) m = umax(m, umax(ov[s].x, ov[s].y));
          if (__all(m != SENT) || dead) break;
          if (spins > SPIN_CAP) dead = true;
          __builtin_amdgcn_s_sleep(BACKOFF);
        }
        const float e0 = __uint_as_float(ov[0].y), e1 = __uint_as_float(ov[1].y), e2 = __uint_as_float(ov[2].y), e3 = __uint_as_float(ov[3].y);
        const float mx = vmax3(vmax(e0, e1), e2, e3);
        const float w0 = __builtin_amdgcn_exp2f(e0 - mx), w1 = __builtin_amdgcn_exp2f(e1 - mx), w2 = __builtin_amdgcn_exp2f(e2 - mx), w3 = __builtin_amdgcn_exp2f(e3 - mx);
        const float inv = __builtin_amdgcn_rcpf((w0 + w1) + (w2 + w3));
        const float oa = (w0 * bflo(ov[0].x) + w1 * bflo(ov[1].x)) + (w2 * bflo(ov[2].x) + w3 * bflo(ov[3].x));
        const float ob = (w0 * bfhi(ov[0].x) + w1 * bfhi(ov[1].x)) + (w2 * bfhi(ov[2].x) + w3 * bfhi(ov[3].x));
        *(LAS u32*)(L + L_IMG + w * 288 + lane * 4) = pk2(oa * inv, ob * inv);
      }
      bar();
      {
        f4 acc[4] = {z, z, z, z};
        v4u of[4];
#pragma unroll
        for (int s = 0; s < 4; ++s) of[s] = *(LAS v4u*)(L + L_IMG + b8 * 288 + s * 64 + gq * 16);
        sched();
#pragma unroll
        for (int s = 0; s < 4; ++s)
#pragma unroll
          for (int t = 0; t < 4; ++t) acc[t] = mfma(Wo[t * 4 + s], of[s], acc[t]);
        const f4 lo = sel4(half, acc[2], acc[0]), hi = sel4(half, acc[3], acc[1]);
        st128(rs, pv_, (l ? WS_P1 : WS_P0) + ps_, v4u{nosent(pk2(lo.x, lo.y)), nosent(pk2(lo.z, lo.w)), nosent(pk2(hi.x, hi.y)), nosent(pk2(hi.z, hi.w))});
      }
      if (l) reanchor(T0, P_P1, P_MARG);
    }
  } else if (r == 31) {
    const bool loadw = w >= 4;
    lptr Lw = L + w * 16384;
    const u64 tag = epoch(a.ws) + 1;
    if (loadw) vmwait<16>(); else vmwait<0>();
    v4u X0, X1;
    ln_input(L, lane, w, X0, X1);
    v4u Wq[16];
    if (loadw) {
      vmwait<12>(); slab64_read1<0>(Lw, lane, Wq);
      vmwait<8>();  slab64_read1<1>(Lw, lane, Wq);
      vmwait<4>();  slab64_read1<2>(Lw, lane, Wq);
      vmwait<0>();  slab64_read1<3>(Lw, lane, Wq);
    } else {
#pragma unroll
      for (int i = 0; i < 16; ++i) Wq[i] = v4u{0u, 0u, 0u, 0u};
      if (w < 2) wait_stage(rs, 0, tag, lane, dead);
    }
    const u32 qv_ = b8 * 2048 + (8 * gq + 4 * half) * 4, qs0 = WS_R + R_BYTES / 2 + xcd * 256 + (w & 1) * 128;
    f4 cs = z;
#pragma unroll 1
    for (int l = 0; l < 2; ++l) {
      if (l) {
        const u32 T0 = wait_t0(rs, tag, dead);
        until(T0, P_X2);
        v4u xv[2];
        gather128<2>(rs, (4 * half + gq) * 128 + b8 * 16, WS_X + X_BYTES + w * 2048, 1024, xv, dead);
        X0 = xv[0]; X1 = xv[1];
      }
      lin_partial(L, Wq, X0, X1, lane, w);
      bar();
      if (w < 2) q_publish(L, rs, lane, w, l, cs, true, qv_, qs0 + l * R_BYTES);
      if (l == 0) {
        lin_colsum_partial(L, Wq, lane, w);
        bar();
        if (w < 2) cs = lin_colsum(L, lane, w);
      }
    }
  } else {
    const int mi = xcd * 7 + (r - 24);
    const v2u bb1 = *(const GAS v2u*)(a.b1 + (64 * mi + 32 * (w & 1) + 8 * gq + 4 * half) * 2);
    const unsigned char* g1_ = a.w1 + (size_t)(128 * w) * (FF * 2) + mi * 128;
    const unsigned char* g2_ = a.w2 + (size_t)(64 * mi + 8 * w) * 2048;
    const u32 ldsw = lds0 + w * 16384;
    lptr Lw = L + w * 16384;
    u64 tag;
    {
      tag = epoch(a.ws) + 1;
      vmwait<0>();
      bar();
      if (tid == 0) st64(rs, 0, WS_F1 + mi * 8, v2u{(u32)tag, (u32)(tag >> 32)});
    }
    const bool slowm = ((mi + (int)((uintptr_t)a.w1 >> 7)) & 7) == 3;
    const bool pacer = slowm && (mi >> 3) == 2;
    until(t_start, slowm ? P_M0S : P_M0);
    slab64_issue4<0, FF * 2>(g1_, ldsw, lane); slab64_issue4<1, FF * 2>(g1_, ldsw, lane);
    slab64_issue4<2, FF * 2>(g1_, ldsw, lane); slab64_issue4<3, FF * 2>(g1_, ldsw, lane);
    v4u W1[16], W2[16];
    vmwait<12>(); slab64_read1<0>(Lw, lane, W1); lgkm0(); rows2k_issue4<0>(g2_, ldsw, lane);
    vmwait<12>(); slab64_read1<1>(Lw, lane, W1); lgkm0(); rows2k_issue4<1>(g2_, ldsw, lane);
    vmwait<12>(); slab64_read1<2>(Lw, lane, W1); lgkm0(); rows2k_issue4<2>(g2_, ldsw, lane);
    vmwait<12>(); slab64_read1<3>(Lw, lane, W1); lgkm0(); rows2k_issue4<3>(g2_, ldsw, lane);
    lin_colsum_partial(L, W1, lane, w);
    bar();
    f4 cs = z;
    if (w < 2) cs = lin_colsum(L, lane, w);
    if (w == 7) wait_stage(rs, 0, tag, lane, dead);
    vmwait<0>();
    bar();
    rows2k_read(L, lane, w, W2);
    const f4 bias1 = {bflo(bb1.x), bfhi(bb1.x), bflo(bb1.y), bfhi(bb1.y)};
    const u32 mv_ = (u32)(4 * half + gq) * 7168 + b8 * 16, ms_ = (u32)(16 * w * 56 + mi) * 128;
    bar();
    u32 T0 = 0;
#pragma unroll 1
    for (int l = 0; l < 2; ++l) {
      if (l) {
        wait_stage(rs, 1, tag, lane, dead);
        T0 = wait_t0(rs, tag, dead); until(T0, P_X1);
      }
      v4u xv[2];
      gather128<2>(rs, (4 * half + gq) * 128 + b8 * 16, WS_X + (2 * l) * X_BYTES + w * 2048, 1024, xv, dead);
      if (l == 0 && pacer && w == 7 && lane == 0) st64(rs, 0, WS_T0, v2u{now(), (u32)tag});
      lin_partial(L, W1, xv[0], xv[1], lane, w);
      bar();
      if (w < 2) {
        float mean, var;
        const f4 acc = lin_final(L, lane, w, mean, var);
        const float r1 = __builtin_amdgcn_rsqf(var + 1e-5f);
        const float r2 = __builtin_amdgcn_rsqf(var * r1 * r1 + 1e-5f);
        const f4 zz = (acc - mean * cs) * (r1 * r2) + bias1;
        float hv[4];
#pragma unroll
        for (int i = 0; i < 4; ++i) {
          const float t = zz[i];
          const float uu = t * (1.0f + 0.044715f * t * t) * (-2.0f * 0.7978845608028654f * 1.4426950408889634f);
          hv[i] = t * __builtin_amdgcn_rcpf(1.0f + __builtin_amdgcn_exp2f(uu));
        }
        *(LAS v2u*)(L + L_IMG + b8 * 160 + (32 * w + 8 * gq + 4 * half) * 2) = v2u{pk2(hv[0], hv[1]), pk2(hv[2], hv[3])};
      }
      bar();
      {
        const v4u h0 = *(LAS v4u*)(L + L_IMG + b8 * 160 + gq * 16), h1 = *(LAS v4u*)(L + L_IMG + b8 * 160 + 64 + gq * 16);
        sched();
#pragma unroll
        for (int uu = 0; uu < 2; ++uu) {
          f4 acc[4];
#pragma unroll
          for (int t = 0; t < 4; ++t) { acc[t] = mfma(W2[(uu * 4 + t) * 2], h0, z); acc[t] = mfma(W2[(uu * 4 + t) * 2 + 1], h1, acc[t]); }
          const f4 lo = sel4(half, acc[2], acc[0]), hi = sel4(half, acc[3], acc[1]);
          st128(rs, mv_, (l ? WS_M1 : WS_M0) + ms_ + uu * (8 * 7168),
                v4u{nosent(pk2(lo.x, lo.y)), nosent(pk2(lo.z, lo.w)), nosent(pk2(hi.x, hi.y)), nosent(pk2(hi.z, hi.w))});
        }
      }
    }
  }
}
}

extern "C" void kernel_launch(void* const* d_in, const int* in_sizes, int n_in, void* d_out, int out_size, void* d_ws, size_t ws_size,
                              hipStream_t stream) {
  Args a;
  a.x = (const unsigned char*)d_in[0];
  a.kc = (const unsigned char*)d_in[1];
  a.vc = (const unsigned char*)d_in[2];
  a.wq = (const unsigned char*)d_in[3];
  a.wo = (const unsigned char*)d_in[4];
  a.w1 = (const unsigned char*)d_in[6];
  a.b1 = (const unsigned char*)d_in[7];
  a.w2 = (const unsigned char*)d_in[8];
  a.b2 = (const unsigned char*)d_in[9];
  a.out = (unsigned char*)d_out;
  a.ws = (unsigned char*)d_ws;
  (void)hipFuncSetAttribute((const void*)fused_decode, hipFuncAttributeMaxDynamicSharedMemorySize, LDS_BYTES);
  hipLaunchKernelGGL(fused_decode, dim3(256), dim3(512), LDS_BYTES, stream, a);
}
```
